```python
import torch

def _softmax(x, axis=-1):
    return torch.softmax(x, dim=axis)
def _gelu(x):
    return torch.nn.functional.gelu(x, approximate="tanh")
def _silu(x):
    return torch.nn.functional.silu(x)
def _sigmoid(x):
    return torch.sigmoid(x)
def _layernorm(x, axis=-1, eps=1e-5):
    m = x.mean(dim=axis, keepdim=True)
    v = x.var(dim=axis, keepdim=True, unbiased=False)
    return (x - m) / torch.sqrt(v + eps)
def _scatter_add(tgt, idx, src, axis=0):
    return tgt.clone().index_add_(axis, idx, src)
def _segment_sum(src, seg, n):
    out = torch.zeros((n,) + src.shape[1:], dtype=src.dtype)
    return out.index_add_(0, seg, src)
def _pad(x, pw):
    flat = tuple(v for pair in reversed(pw) for v in pair)
    return torch.nn.functional.pad(x, flat)

kernel_name = 'gated_mix__ssm_conv_s4d__mha_block__prompt'

def setup_inputs():

    _w = (lambda a: (lambda t: t.to(torch.bfloat16) if t.is_floating_point() else t)(torch.as_tensor(a)))
    def _A_setup():
        Bsz, L, D, N = 1, 2048, 576, 128
        return {
            "u": _w((lambda *s: torch.randn(*s, dtype=torch.bfloat16) * 0.05)(Bsz, L, D)),
            "log_A_real": _w((lambda *s: torch.randn(*s, dtype=torch.bfloat16) * 6.4)(D, N) * 0.5),
            "A_imag": _w((lambda *s: torch.randn(*s, dtype=torch.bfloat16) * 0.1)(D, N) * 3.14),
            "C_real": _w((lambda *s: torch.randn(*s, dtype=torch.bfloat16) * 0.4)(D, N) / N ** 0.5),
            "C_imag": _w((lambda *s: torch.randn(*s, dtype=torch.bfloat16) * 0.4)(D, N) / N ** 0.5),
            "log_dt": _w((lambda *s: torch.randn(*s, dtype=torch.bfloat16) * 0.1)(D) * 0.5 - 4.0),
            "D_skip": _w((lambda *s: torch.randn(*s, dtype=torch.bfloat16) * 0.1)(D)),
        }
    _A_in = _A_setup()

    def _B_setup():
        B, S, D, H = 1, 2048, 576, 32
        return {
            "x": _w((lambda *s: torch.randn(*s, dtype=torch.bfloat16) * 0.05)(B, S, D)),
            "wqkv": _w((lambda *s: torch.randn(*s, dtype=torch.bfloat16) * 0.8)(D, 3 * D)),
            "wo": _w((lambda *s: torch.randn(*s, dtype=torch.bfloat16) * 0.05)(D, D)),
        }
    _B_in = _B_setup()

    return {
        "A_u": _A_in["u"],
        "A_log_A_real": _A_in["log_A_real"],
        "A_A_imag": _A_in["A_imag"],
        "A_C_real": _A_in["C_real"],
        "A_C_imag": _A_in["C_imag"],
        "A_log_dt": _A_in["log_dt"],
        "A_D_skip": _A_in["D_skip"],
        "B_x": _B_in["x"],
        "B_wqkv": _B_in["wqkv"],
        "B_wo": _B_in["wo"],
        "gate_w": _w((lambda *s: torch.randn(*s, dtype=torch.bfloat16) * 0.05)(576) * 16.0),
    }

def reference(A_u, A_log_A_real, A_A_imag, A_C_real, A_C_imag, A_log_dt, A_D_skip, B_x, B_wqkv, B_wo, gate_w):
    def _A_ref(u, log_A_real, A_imag, C_real, C_imag, log_dt, D_skip):
        L = u.shape[1]
        dt = torch.exp(log_dt)
        A = -torch.exp(log_A_real) + 1j * A_imag
        C = (C_real + 1j * C_imag) * dt[:, None]
        pos = (lambda x, dt: x.to(dt))(torch.arange(L), log_dt.dtype)
        P = torch.exp((A * dt[:, None])[:, :, None] * pos[None, None, :])
        K = 2.0 * torch.einsum("dn,dnl->dl", C, P).real
        L2 = 2 * L
        u_t = (lambda x, *d: x.permute(*d))(u, 0, 2, 1)
        Kf = (lambda x, *a, **k: torch.fft.rfft(x.float(), *a, **k))(K, L2)
        Uf = (lambda x, *a, **k: torch.fft.rfft(x.float(), *a, **k))(u_t, L2)
        y = (lambda x, *a, **k: torch.fft.irfft(x, *a, **k).to(torch.bfloat16))(Uf * Kf[None, :, :], L2)[:, :, :L]
        y = (lambda x, *d: x.permute(*d))(y, 0, 2, 1)
        return y + u * D_skip[None, None, :]

    def _B_ref(x, wqkv, wo):
        B, S, D = x.shape
        H = 32
        Dh = D // H
        qkv = (lambda x, *d: x.permute(*d))((x @ wqkv).reshape(B, S, 3, H, Dh), 2, 0, 3, 1, 4)
        q, k, v = qkv[0], qkv[1], qkv[2]
        scores = torch.einsum("bhqd,bhkd->bhqk", q, k) * (Dh ** -0.5)
        p = _softmax(scores, axis=-1)
        o = torch.einsum("bhqk,bhkd->bhqd", p, v)
        return (lambda x, *d: x.permute(*d))(o, 0, 2, 1, 3).reshape(B, S, D) @ wo

    x = A_u
    g = _sigmoid((x * gate_w).sum(-1, keepdims=True))
    a = _A_ref(x, A_log_A_real, A_A_imag, A_C_real, A_C_imag, A_log_dt, A_D_skip)
    b = _B_ref(x, B_wqkv, B_wo)
    return g * a + (1 - g) * b
```

```cpp
#include <hip/hip_runtime.h>
#include <hip/hip_bf16.h>
#include <stdint.h>

constexpr int SEQ = 2048, DM = 576, NMODE = 128, NHEAD = 32;
constexpr float QSCALE = 0.23570226039551584f * 1.4426950408889634f;

constexpr int KV_CHUNK = 20480, KV_K2 = 8192, KV_V = 10240, KV_HEAD = 8 * KV_CHUNK;
constexpr unsigned WS_KV = 0;
constexpr unsigned WS_Q1 = WS_KV + NHEAD * KV_HEAD;
constexpr unsigned WS_Q2 = WS_Q1 + NHEAD * 65536;
constexpr unsigned WS_UT = WS_Q2 + NHEAD * 16384;
constexpr unsigned WS_G  = WS_UT + DM * 4096;
constexpr unsigned WS_O1 = WS_G + 8192;
constexpr unsigned WS_O2 = WS_O1 + NHEAD * 65536;
constexpr unsigned WS_AT = WS_O2 + NHEAD * 8192;
constexpr unsigned WS_FL = WS_AT + DM * 4096;
constexpr unsigned FL_EPOCH = WS_FL, FL_ST = WS_FL + 128;
constexpr unsigned FL_FP = FL_ST + 2048;
constexpr unsigned FL_FK = FL_FP + 12 * 128;
constexpr unsigned FL_F3 = FL_FK + 512;
constexpr unsigned FL_F2 = FL_F3 + NHEAD * 128;
constexpr unsigned WS_END = FL_F2 + 8 * 256;

typedef float f32x2 __attribute__((ext_vector_type(2)));
typedef float f32x4 __attribute__((ext_vector_type(4)));
typedef float f32x16 __attribute__((ext_vector_type(16)));
typedef float f32x32 __attribute__((ext_vector_type(32)));
typedef unsigned u32x2 __attribute__((ext_vector_type(2)));
typedef unsigned u32x4 __attribute__((ext_vector_type(4)));
typedef unsigned u32x8 __attribute__((ext_vector_type(8)));
typedef short s16x4 __attribute__((ext_vector_type(4)));
typedef __bf16 bf16x8 __attribute__((ext_vector_type(8)));
typedef unsigned long long u64;
typedef __attribute__((address_space(3))) char* lds_ptr;
typedef __attribute__((address_space(3))) const char* lds_cptr;
typedef __attribute__((address_space(1))) u64 gu64;
typedef __amdgpu_buffer_rsrc_t rsrc_t;
#define LDS_AS __attribute__((address_space(3)))
#define RLX_AGENT __ATOMIC_RELAXED, __HIP_MEMORY_SCOPE_AGENT
constexpr int AUX_SC1 = 16;

__device__ __forceinline__ float bf2f(unsigned short b) { return __uint_as_float((unsigned)b << 16); }
typedef __bf16 bf16x2 __attribute__((ext_vector_type(2)));
__device__ __forceinline__ unsigned cvtpk(float lo, float hi) {
    return __builtin_bit_cast(unsigned, __builtin_convertvector(f32x2{lo, hi}, bf16x2));
}
__device__ __forceinline__ float bfround(float x) { return __uint_as_float(cvtpk(x, x) << 16); }
__device__ __forceinline__ float bflo(unsigned u) { return __uint_as_float(u << 16); }
__device__ __forceinline__ float bfhi(unsigned u) { return __uint_as_float(u & 0xffff0000u); }

__device__ __forceinline__ f32x4 mfma16(u32x4 a, u32x4 b, f32x4 c) {
    return __builtin_amdgcn_mfma_f32_16x16x32_bf16(__builtin_bit_cast(bf16x8, a), __builtin_bit_cast(bf16x8, b), c, 0, 0, 0);
}
__device__ __forceinline__ f32x16 mfma32(u32x4 a, u32x4 b, f32x16 c) {
    return __builtin_amdgcn_mfma_f32_32x32x16_bf16(__builtin_bit_cast(bf16x8, a), __builtin_bit_cast(bf16x8, b), c, 0, 0, 0);
}
__device__ __forceinline__ f32x16 mfma32k8(u32x2 a, u32x2 b, f32x16 c) {
    return __builtin_amdgcn_mfma_f32_32x32x8bf16_1k(__builtin_bit_cast(s16x4, a), __builtin_bit_cast(s16x4, b), c, 0, 0, 0);
}
__device__ __forceinline__ u32x4 lds_rd128(lds_cptr p) { return *(const LDS_AS u32x4*)p; }
__device__ __forceinline__ void lds_wr128(lds_ptr p, u32x4 v) { *(LDS_AS u32x4*)p = v; }
__device__ __forceinline__ u32x2 lds_tr(lds_cptr p) {
    return __builtin_bit_cast(u32x2, __builtin_amdgcn_ds_read_tr16_b64_v4i16((LDS_AS s16x4*)p));
}
__device__ __forceinline__ void glds16(const void* sbase, unsigned voff, unsigned lds_dst) {
    unsigned keep;
    asm volatile("s_mov_b32 %0, m0\n\ts_mov_b32 m0, %3\n\ts_nop 0\n\tglobal_load_lds_dwordx4 %1, %2\n\ts_mov_b32 m0, %0"
                 : "=&s"(keep) : "v"(voff), "s"(sbase), "s"(lds_dst) : "memory");
}
__device__ __forceinline__ u32x4 ld16(rsrc_t r, unsigned voff, unsigned soff) { return __builtin_amdgcn_raw_buffer_load_b128(r, voff, soff, AUX_SC1); }
__device__ __forceinline__ unsigned ld4(rsrc_t r, unsigned voff, unsigned soff) { return __builtin_amdgcn_raw_buffer_load_b32(r, voff, soff, AUX_SC1); }
__device__ __forceinline__ void st16(rsrc_t r, unsigned voff, unsigned soff, u32x4 v) { __builtin_amdgcn_raw_buffer_store_b128(v, r, voff + soff, 0, AUX_SC1); }
__device__ __forceinline__ void st8(rsrc_t r, unsigned voff, unsigned soff, u32x2 v) { __builtin_amdgcn_raw_buffer_store_b64(v, r, voff, soff, AUX_SC1); }
__device__ __forceinline__ void st4(rsrc_t r, unsigned voff, unsigned soff, unsigned v) { __builtin_amdgcn_raw_buffer_store_b32(v, r, voff, soff, AUX_SC1); }
__device__ __forceinline__ void flag_set(char* ws, unsigned off, u64 tag) { __hip_atomic_store((gu64*)(ws + off), tag, RLX_AGENT); }
__device__ __forceinline__ void flag_wait(char* ws, unsigned off, bool active, u64 tag) {
    for (unsigned spins = 0; spins < (1u << 17); ++spins) {
        const u64 v = active ? __hip_atomic_load((gu64*)(ws + off), RLX_AGENT) : tag;
        if (__all(v == tag)) return;
        __builtin_amdgcn_s_sleep(1);
    }
}
#define DRAIN_VM() asm volatile("s_waitcnt vmcnt(0)" ::: "memory")
#define BARRIER() do { __builtin_amdgcn_s_waitcnt(0xC07F); asm volatile("s_barrier" ::: "memory"); } while (0)
#define WAIT_VM_BARRIER(N) asm volatile("s_waitcnt vmcnt(" #N ") lgkmcnt(0)\n\ts_barrier" ::: "memory")
__device__ __forceinline__ int lane_id() { int l; asm volatile("v_mbcnt_lo_u32_b32 %0, -1, 0\n\tv_mbcnt_hi_u32_b32 %0, -1, %0" : "=v"(l)); return l; }
__device__ __forceinline__ int uni(int x) { return __builtin_amdgcn_readfirstlane(x); }

#define PROBE(n) do { } while (0)
#define STAMP(idx) do { } while (0)

constexpr int P_SLOT = 34816, P_X = 0, P_W = 16384;
constexpr int L_STAGE = P_SLOT, L_STAGEG = L_STAGE + 8 * 5120;
constexpr int L_UTILE = 0;
constexpr int L_RING = 0;
constexpr int CK_SZ = 4352, CU_SZ = 4736;
constexpr int L_KGT = 88960;
constexpr int L_SCR = L_KGT;
constexpr int L_CK2 = 61440, L_CU2 = L_CK2 + CK_SZ;
constexpr int L_CK0 = L_KGT + 3 * (9280 + 8448), L_CU0 = L_CK0 + 2 * CK_SZ;
constexpr int L_SPILL = L_CU0 + 2 * CU_SZ;
constexpr int L_KGP = 20480;
__device__ __forceinline__ int l_ck(int j) { return j == 2 ? L_CK2 : L_CK0 + j * CK_SZ; }
__device__ __forceinline__ int l_cu(int j) { return j == 2 ? L_CU2 : L_CU0 + j * CU_SZ; }
__device__ __forceinline__ int l_kgt(int w) { return L_KGT + (w >> 1) * (9280 + 8448) + (w & 1) * 9280; }
constexpr int WO_HALF = 288 * 144;
constexpr int L_WO = 61440, L_AT = L_WO + 2 * WO_HALF + 64;
constexpr int L_RED = 20480;
constexpr int L_CNT = L_SPILL + 384, L_GATE = L_CNT + 64;
constexpr int LDS_BYTES = L_GATE + 1280;
static_assert(4 * P_SLOT <= L_CNT && L_SCR + 8 * 4608 <= L_CK0 && L_KGT >= L_STAGEG + 64 && L_AT + 80 * 128 <= L_CNT && LDS_BYTES <= 163840, "LDS map");

__device__ __forceinline__ void lds_arrive(lds_ptr lds, int cnt_idx, int lane) {
    __builtin_amdgcn_s_waitcnt(0xC07F);
    asm volatile("" ::: "memory");
    if (lane == 0) __hip_atomic_fetch_add((LDS_AS unsigned*)(lds + L_CNT + cnt_idx * 4), 1u, __ATOMIC_RELAXED, __HIP_MEMORY_SCOPE_WORKGROUP);
}
__device__ __forceinline__ void lds_await(lds_ptr lds, int cnt_idx, unsigned n) {
    for (unsigned spins = 0; spins < (1u << 24); ++spins) {
        unsigned v;
        asm volatile("ds_read_b32 %0, %1\n\ts_waitcnt lgkmcnt(0)" : "=v"(v) : "v"((unsigned)(uintptr_t)lds + L_CNT + cnt_idx * 4) : "memory");
        if (__builtin_amdgcn_readfirstlane(v) >= n) break;
        __builtin_amdgcn_s_sleep(1);
    }
}
__device__ __forceinline__ void publish(char* ws, lds_ptr lds, int cnt_idx, unsigned n, unsigned flag_off, u64 tag, int lane) {
    DRAIN_VM();
    if (lane == 0) {
        const unsigned k = __hip_atomic_fetch_add((LDS_AS unsigned*)(lds + L_CNT + cnt_idx * 4), 1u, __ATOMIC_RELAXED, __HIP_MEMORY_SCOPE_WORKGROUP);
        if (k == n - 1) flag_set(ws, flag_off, tag);
    }
}

struct Args {
    const char* x; const char* wqkv; const char* wo; const char* gate;
    const unsigned short *lar, *aim, *cre, *cim, *ldt, *dsk;
    char* ws; char* out;
};

__device__ __forceinline__ void l2_warm_addr(const Args& a, int xcd, int slot, int w, int lane, const char*& p0, const char*& p1) {
    const bool tr = slot < 8;
    const int s0 = tr ? 4 : 2, ns = tr ? 5 : 2, nx = 512 * ns, nw = 64 * ns * 14;
    auto addr = [&](int i) {
        const int k = i - nx, row = k / 14;
        return i < nx ? a.x + ((xcd >> 1) * 512 + i / ns) * 1152 + (s0 + i % ns) * 128
                      : a.wqkv + (64 * s0 + row) * 3456 + (xcd & 1) * 1664 + (k - 14 * row) * 128;
    };
    const int base = tr ? slot * 880 + w * 110 : ((slot - 8) * 2 + (w & 1)) * 59;
    const int i0 = base + (tr ? lane : (w < 2 && lane < 59 ? lane : 0)), i1 = base + (tr && lane < 46 ? lane + 64 : 0);
    p0 = addr(i0 < nx + nw ? i0 : base < nx + nw ? base : 0);
    p1 = addr(i1 < nx + nw ? i1 : 0);
}

__device__ __forceinline__ void gemm_loader(const Args& a, lds_ptr lds, int mi, int nj, int hw, int lane, char* dbg_ws) {
    const unsigned lds0 = (unsigned)(uintptr_t)lds;
    const unsigned xoff = (mi * 128 + 32 * hw + (lane >> 3)) * 1152 + (((lane & 7) ^ ((lane >> 3) & 7)) << 4);
    auto issue_x = [&](int s) {
        const unsigned slot = lds0 + (s & 3) * P_SLOT;
#pragma unroll
        for (int e = 0; e < 4; ++e) glds16(a.x + s * 128 + e * 9216, xoff, slot + P_X + (4 * hw + e) * 1024);
    };
    issue_x(0);
    __builtin_amdgcn_sched_barrier(0);
    unsigned woff[5];
#pragma unroll
    for (int e = 0; e < 5; ++e) {
        const int c = 288 * hw + 64 * e + (e == 4 ? (lane & 31) : lane), rho = c / 18, cc = c - rho * 18, k = rho ^ (((rho >> 3) & 1) << 2);
        woff[e] = k * 3456 + nj * 288 + cc * 16;
    }
    auto issue_w = [&](int s) {
        const unsigned slot = lds0 + (s & 3) * P_SLOT;
        const char* wsrc = a.wqkv + (size_t)s * (64 * 3456);
#pragma unroll
        for (int e = 0; e < 4; ++e) glds16(wsrc, woff[e], slot + P_W + (288 * hw + 64 * e) * 16);
        if (lane < 32) glds16(wsrc, woff[4], slot + P_W + (288 * hw + 256) * 16);
    };
    auto issue = [&](int s) { issue_x(s); issue_w(s); };
    if (nj < 8 && hw < 2) { if (hw == 0 || lane < 8) glds16(a.gate, (hw * 64 + lane) * 16, lds0 + L_GATE + hw * 1024); }
#define PST(i)
    issue_w(0); issue(1);
    PST(1);
    WAIT_VM_BARRIER(9);
    PST(2);
    issue(2); issue(3);
    PST(3);
#pragma unroll
    for (int s = 1; s < 9; ++s) {
        if (s <= 6) WAIT_VM_BARRIER(18); else if (s == 7) WAIT_VM_BARRIER(9); else WAIT_VM_BARRIER(0);
        PST(2 + 2 * s);
        if (s + 3 <= 8) issue(s + 3);
        PST(3 + 2 * s);
    }
    (void)dbg_ws;
}

template <bool VT, int CH>
__device__ __forceinline__ void gemm_wave(const Args& a, rsrc_t rs, lds_ptr lds, int mi, int nj, int w, int lane, char* dbg_ws) {
    constexpr int NT = CH == 0 ? 5 : 4;
    const int tg = w & 3, g = lane >> 4, r = lane & 15, q = r >> 2, p = lane & 3;
    const int tok0 = mi * 128;
    const int x0 = (32 * tg + r) * 128 + ((g ^ (r & 7)) << 4), x1 = x0 ^ 64;
    int wrow[2];
#pragma unroll
    for (int hf = 0; hf < 2; ++hf) wrow[hf] = P_W + (8 * g + q + 4 * (hf ^ (g & 1))) * 288;
    const int cbase = VT ? (8 * p + (CH ? 160 : 0)) : (((p >> 1) * 20 + 8 * (p & 1)) * 2 + (CH ? 144 : 0));
    const int rbase = 72 * p + 32;
    const bool do_gate = (CH == 1) && nj < 8 && (tg == (nj >> 1));
    const int gtt = nj & 1;
    f32x4 acc[2][NT]; f32x4 gacc = {0.f, 0.f, 0.f, 0.f};
#pragma unroll
    for (int i = 0; i < NT; ++i) { acc[0][i] = gacc; acc[1][i] = gacc; }
    struct Frag { u32x4 x0, x1, w[NT], gt; };
    auto fetch = [&](Frag& f, int s, int ks) {
        lds_cptr sl = (lds_cptr)lds + (s & 3) * P_SLOT;
        f.x0 = lds_rd128(sl + (ks ? x1 : x0)); f.x1 = lds_rd128(sl + (ks ? x1 : x0) + 2048);
#pragma unroll
        for (int i = 0; i < NT; ++i) {
            const int co = (!VT && CH == 0 && i == 4) ? rbase : (cbase + (VT ? 32 * i : 72 * (i >> 1) + 8 * (i & 1)));
            const u32x2 lo = lds_tr(sl + wrow[0] + co + ks * 9216), hi = lds_tr(sl + wrow[1] + co + ks * 9216);
            f.w[i] = u32x4{lo[0], lo[1], hi[0], hi[1]};
        }
        if (CH == 1) f.gt = lds_rd128((lds_cptr)lds + L_GATE + s * 128 + ks * 64 + g * 16);
    };
    auto mma = [&](const Frag& f) {
#pragma unroll
        for (int i = 0; i < NT; ++i) {
            if (VT) { acc[0][i] = mfma16(f.x0, f.w[i], acc[0][i]); acc[1][i] = mfma16(f.x1, f.w[i], acc[1][i]); }
            else    { acc[0][i] = mfma16(f.w[i], f.x0, acc[0][i]); acc[1][i] = mfma16(f.w[i], f.x1, acc[1][i]); }
        }
        if (CH == 1) gacc = mfma16(gtt ? f.x1 : f.x0, f.gt, gacc);
    };
    Frag f0, f1;
    BARRIER();
    STAMP(1);
    fetch(f0, 0, 0);
#pragma unroll 1
    for (int s = 0; s < 8; ++s) {
        fetch(f1, s, 1);
        __builtin_amdgcn_sched_barrier(0);
        mma(f0);
        __builtin_amdgcn_sched_barrier(0);
        BARRIER();
        fetch(f0, s + 1, 0);
        __builtin_amdgcn_sched_barrier(0);
        mma(f1);
        __builtin_amdgcn_sched_barrier(0);
    }
    fetch(f1, 8, 1);
    mma(f0);
    mma(f1);
    STAMP(2);
    lds_ptr stg = lds + L_STAGE;
    if (CH == 1 && do_gate && r == 0) {
        f32x4 o;
#pragma unroll
        for (int i = 0; i < 4; ++i) { const float z = bfround(gacc[i]); o[i] = bfround(1.0f / (1.0f + __expf(-z))); }
        *(LDS_AS f32x4*)(lds + L_STAGEG + g * 16) = o;
    }
    if (VT) {
        const int base = (2 * tg + (lane >> 5)) * 640 + (g & 1) * 320;
#pragma unroll
        for (int i = 0; i < NT; ++i) {
            const int col = 16 * (i + (CH ? 5 : 0)) + r, hd = col / 18, dv = col - 18 * hd;
            const unsigned a0 = cvtpk(acc[0][i][0], acc[0][i][1]), a1 = cvtpk(acc[0][i][2], acc[0][i][3]);
            const unsigned b0 = cvtpk(acc[1][i][0], acc[1][i][1]), b1 = cvtpk(acc[1][i][2], acc[1][i][3]);
            const auto s0 = __builtin_amdgcn_permlane32_swap(a0, b0, false, false);
            const auto s1 = __builtin_amdgcn_permlane32_swap(a1, b1, false, false);
            lds_wr128(stg + hd * 5120 + base + dv * 16, u32x4{s0[0], s1[0], s0[1], s1[1]});
        }
        if (CH == 0) {
            const int hd = lane >> 3, st = (lane >> 2) & 1, h2 = (lane >> 1) & 1, row = lane & 1;
            const unsigned v = row ? 0u : 0x3f803f80u;
            lds_wr128(stg + hd * 5120 + (2 * tg + st) * 640 + h2 * 320 + (18 + row) * 16, u32x4{v, v, v, v});
        }
    } else {
        const float sc = nj < 4 ? QSCALE : 1.0f;
        const int hh = g & 1;
#pragma unroll
        for (int tt = 0; tt < 2; ++tt) {
            const int t = 16 * tt + r;
#pragma unroll
            for (int pp = 0; pp < 2; ++pp) {
                const int hd = 2 * (2 * CH + pp) + (g >> 1);
                const f32x4 u = acc[tt][2 * pp], v = acc[tt][2 * pp + 1];
                lds_wr128(stg + hd * 5120 + tg * 1024 + hh * 512 + t * 16,
                          u32x4{cvtpk(u[0] * sc, u[1] * sc), cvtpk(u[2] * sc, u[3] * sc), cvtpk(v[0] * sc, v[1] * sc), cvtpk(v[2] * sc, v[3] * sc)});
            }
            if (CH == 0) {
                const f32x4 u = acc[tt][NT - 1];
#pragma unroll
                for (int e = 0; e < 2; ++e)
                    *(LDS_AS u32x2*)(stg + (2 * g + e) * 5120 + 4096 + (32 * tg + t) * 8) = u32x2{cvtpk(u[2 * e] * sc, u[2 * e + 1] * sc), 0u};
            }
        }
    }
    BARRIER();
}

__device__ __forceinline__ void gemm_store(rsrc_t rs, lds_ptr lds, int mi, int nj, int hw, int lane) {
    BARRIER();
    u32x4 v[10];
#pragma unroll
    for (int i = 0; i < 10; ++i) v[i] = lds_rd128((lds_cptr)lds + L_STAGE + hw * 10240 + i * 1024 + lane * 16);
    const u32x4 gv = lds_rd128((lds_cptr)lds + L_STAGEG + (lane & 3) * 16);
    const bool late = nj >= 4 && mi >= 6;
    if (late) BARRIER();
#pragma unroll
    for (int i = 0; i < 10; ++i) {
        const int hd = (nj & 3) * 8 + 2 * hw + i / 5, pc = i % 5;
        unsigned dst;
        if (nj < 4) dst = pc < 4 ? WS_Q1 + hd * 65536 + (4 * mi + pc) * 1024 : WS_Q2 + hd * 16384 + mi * 1024;
        else {
            const unsigned cb = WS_KV + hd * KV_HEAD + (mi >> 1) * KV_CHUNK;
            dst = nj < 8 ? (pc < 4 ? cb + (4 * (mi & 1) + pc) * 1024 : cb + KV_K2 + (mi & 1) * 1024) : cb + KV_V + (mi & 1) * 5120 + pc * 1024;
        }
        st16(rs, lane * 16, dst, v[i]);
    }
    if (nj < 8 && hw == 0 && lane < 4) st16(rs, (mi * 128 + 32 * (nj >> 1) + 16 * (nj & 1)) * 4 + lane * 16, WS_G, gv);
    if (!late) BARRIER();
}

__device__ __forceinline__ f32x2 cmul(f32x2 a, f32x2 b) {
    f32x2 t;
    asm("v_pk_mul_f32 %0, %1, %2 op_sel:[0,0] op_sel_hi:[0,1]" : "=v"(t) : "v"(a), "v"(b));
    asm("v_pk_fma_f32 %0, %1, %2, %0 op_sel:[1,1,0] op_sel_hi:[1,0,1] neg_lo:[0,1,0]" : "+v"(t) : "v"(a), "v"(b));
    return t;
}
__device__ __forceinline__ f32x2 zpow(float lam, float om, float pw) {
    const float m = __builtin_amdgcn_exp2f(lam * pw), rv = __builtin_amdgcn_fractf(om * pw);
    return f32x2{m * __builtin_amdgcn_cosf(rv), m * __builtin_amdgcn_sinf(rv)};
}
struct KgPar { unsigned short lar, aim, cre, cim, ldt; };
__device__ __forceinline__ f32x16 kgen_half(const KgPar& in, lds_ptr tab, int lane) {
    f32x16 acc = {};
    const float dt = bfround(expf(bf2f(in.ldt)));
    const float ar = -bfround(expf(bf2f(in.lar)));
    const float lam = (ar * dt) * 1.4426950408889634f, om = (bf2f(in.aim) * dt) * 0.15915494309189535f;
    const f32x2 c2 = {2.0f * (bf2f(in.cre) * dt), 2.0f * (bf2f(in.cim) * dt)};
    lds_ptr wr = tab + (lane >> 3) * 1056 + ((lane >> 2) & 1) * 528 + (lane & 3) * 4;
    lds_cptr rd = (lds_cptr)tab + (lane >> 5) * 528 + (lane & 31) * 16;
    auto put = [&](int row, float re, float im) { *(LDS_AS unsigned*)(wr + row * 16) = cvtpk(re, im); };
    const f32x2 z8 = zpow(lam, om, 8.f), z64 = zpow(lam, om, 64.f);
    f32x2 e0 = {1.f, 0.f}, e1 = z64, e2 = cmul(z64, z64), e3 = cmul(e2, z64);
#pragma unroll
    for (int i = 0; i < 8; ++i) {
        put(i, e0[0], e0[1]); put(8 + i, e1[0], e1[1]); put(16 + i, e2[0], e2[1]); put(24 + i, e3[0], e3[1]);
        if (i < 7) { e0 = cmul(e0, z8); e1 = cmul(e1, z8); e2 = cmul(e2, z8); e3 = cmul(e3, z8); }
    }
    u32x4 bf[8];
#pragma unroll
    for (int s = 0; s < 8; ++s) bf[s] = lds_rd128(rd + s * 1056);
    const f32x2 z1 = zpow(lam, om, 1.f), z256 = zpow(lam, om, 256.f);
    const f32x2 z2 = cmul(z1, z1), z4 = cmul(z2, z2);
    f32x2 an[9]; an[0] = c2;
#pragma unroll
    for (int j = 1; j < 9; ++j) an[j] = cmul(an[j - 1], z256);
    f32x2 f = c2;
    put(0, f[0], -f[1]);
#pragma unroll
    for (int i = 1; i < 8; ++i) { f = cmul(f, z1); put(i, f[0], -f[1]); }
    f = an[1]; put(8, f[0], -f[1]);
#pragma unroll
    for (int i = 1; i < 4; ++i) { f = cmul(f, z2); put(8 + i, f[0], -f[1]); }
    put(12, an[2][0], -an[2][1]); f = cmul(an[2], z4); put(13, f[0], -f[1]);
    put(14, an[3][0], -an[3][1]); f = cmul(an[3], z4); put(15, f[0], -f[1]);
#pragma unroll
    for (int j = 4; j < 9; ++j) put(12 + j, an[j][0], -an[j][1]);
#pragma unroll
    for (int s = 0; s < 8; ++s) acc = mfma32(lds_rd128(rd + s * 1056), bf[s], acc);
    return acc;
}
__device__ __forceinline__ int kidx(int p) { return (p + 4 * (p >> 5)) * 4; }
__device__ __forceinline__ void kgen_expand(const f32x16& acc, lds_ptr kb, lds_ptr tab, int lane) {
    const int b8 = (lane & 31) * 8, hh = lane >> 5;
    auto st = [&](int p, float v) { *(LDS_AS float*)(tab + kidx(p)) = v; };
#pragma unroll
    for (int i = 0; i < 4; ++i) {
        st(4 * hh + i + b8, acc[i]);
        st((hh ? 512 + (i >> 1) * 256 + (i & 1) * 4 : 256 + 2 * i) + b8, acc[4 + i]);
    }
    if (hh == 0) {
#pragma unroll
        for (int i = 0; i < 4; ++i) st(1024 + 256 * i + b8, acc[8 + i]);
    } else if (b8 == 0) st(2048, acc[8]);
    auto ld = [&](int p) { return *(const LDS_AS float*)(tab + kidx(p)); };
    lds_ptr kc = kb + 128;
    if (lane < 16) lds_wr128(kb + (lane < 8 ? lane * 16 : 4224 + (lane - 8) * 16), u32x4{0u, 0u, 0u, 0u});
    {
        const f32x4 v = *(const LDS_AS f32x4*)(tab + kidx(4 * lane));
        *(LDS_AS u32x2*)(kc + 8 * lane) = u32x2{cvtpk(v[0], v[1]), cvtpk(v[2], v[3])};
        const int l0 = 256 + 4 * lane; const float x = ld(l0), y = ld(l0 + 2), z = ld(l0 + 4);
        *(LDS_AS u32x2*)(kc + 2 * l0) = u32x2{cvtpk(x, x), cvtpk(y, z)};
    }
    {
        const int l0 = 512 + 8 * lane; const float x = ld(l0), y = ld(l0 + 4), z = ld(l0 + 8);
        lds_wr128(kc + 2 * l0, u32x4{cvtpk(x, x), cvtpk(x, y), cvtpk(y, y), cvtpk(z, z)});
    }
#pragma unroll
    for (int m = 0; m < 2; ++m) {
        const int l0 = 1024 + 512 * m + 8 * lane; const float x = ld(l0), z = ld(l0 + 8), mid = (lane & 1) ? z : x;
        lds_wr128(kc + 2 * l0, u32x4{cvtpk(x, x), cvtpk(x, x), cvtpk(mid, z), cvtpk(z, z)});
    }
}

__device__ __forceinline__ void transpose_wave(const Args& a, rsrc_t rs, lds_ptr lds, int pid, int w, int lane) {
    const int tb = pid >> 3, cb = pid & 7;
    if (w >= 8) {
        const unsigned lds0 = (unsigned)(uintptr_t)lds;
#pragma unroll
        for (int e = 0; e < 9; ++e) {
            const int i = (w - 8) + 4 * e, c = 64 * i + lane, row = c / 9, ch = c - 9 * row;
            glds16(a.x, (256 * tb + row) * 1152 + cb * 144 + ch * 16, lds0 + L_UTILE + i * 1024);
        }
        WAIT_VM_BARRIER(0);
    } else {
        BARRIER();
        const int s = w & 3, g = lane >> 4, q = (lane & 15) >> 2, p = lane & 3, ib = 4 * (w >> 2) + g;
#pragma unroll
        for (int cg = 0; cg < 5; ++cg) {
            lds_cptr ap = (lds_cptr)lds + L_UTILE + (4 * (8 * ib + q) + s) * 144 + (16 * cg + 4 * p) * 2;
            const u32x2 lo = lds_tr(ap), hi = lds_tr(ap + 16 * 144);
            const int c = 16 * cg + (lane & 15);
            if (c < 72) st16(rs, (72 * cb + c) * 4096 + s * 1024 + (64 * tb + 8 * ib) * 2, WS_UT, u32x4{lo[0], lo[1], hi[0], hi[1]});
        }
    }
}

#define ATTN_LOOP_ASM \
    "ds_read_b128 v[144:147], v160 offset:0\n\t" \
    "ds_read_b64 v[148:149], v161 offset:8192\n\t" \
    "v_mov_b32 v132, 0\n\t" \
    "v_mov_b32 v133, 0\n\t" \
    "v_mov_b32 v134, 0\n\t" \
    "v_mov_b32 v135, 0\n\t" \
    "v_mov_b32 v140, 0\n\t" \
    "v_mov_b32 v141, 0\n\t" \
    "v_mov_b32 v142, 0\n\t" \
    "v_mov_b32 v143, 0\n\t" \
    "v_mov_b32 v88, 0\n\t" \
    "v_mov_b32 v89, 0\n\t" \
    "v_mov_b32 v90, 0\n\t" \
    "v_mov_b32 v91, 0\n\t" \
    "v_mov_b32 v92, 0\n\t" \
    "v_mov_b32 v93, 0\n\t" \
    "v_mov_b32 v94, 0\n\t" \
    "v_mov_b32 v95, 0\n\t" \
    "v_mov_b32 v96, 0\n\t" \
    "v_mov_b32 v97, 0\n\t" \
    "v_mov_b32 v98, 0\n\t" \
    "v_mov_b32 v99, 0\n\t" \
    "v_mov_b32 v100, 0\n\t" \
    "v_mov_b32 v101, 0\n\t" \
    "v_mov_b32 v102, 0\n\t" \
    "v_mov_b32 v103, 0\n\t" \
    "v_mov_b32 v104, 0\n\t" \
    "v_mov_b32 v105, 0\n\t" \
    "v_mov_b32 v106, 0\n\t" \
    "v_mov_b32 v107, 0\n\t" \
    "v_mov_b32 v108, 0\n\t" \
    "v_mov_b32 v109, 0\n\t" \
    "v_mov_b32 v110, 0\n\t" \
    "v_mov_b32 v111, 0\n\t" \
    "v_mov_b32 v112, 0\n\t" \
    "v_mov_b32 v113, 0\n\t" \
    "v_mov_b32 v114, 0\n\t" \
    "v_mov_b32 v115, 0\n\t" \
    "v_mov_b32 v116, 0\n\t" \
    "v_mov_b32 v117, 0\n\t" \
    "v_mov_b32 v118, 0\n\t" \
    "v_mov_b32 v119, 0\n\t" \
    "s_waitcnt lgkmcnt(0)\n\t" \
    "v_mfma_f32_32x32x16_bf16 v[24:39], v[144:147], v[150:153], 0\n\t" \
    "v_mfma_f32_32x32x16_bf16 v[40:55], v[144:147], v[154:157], 0\n\t" \
    "ds_read_b128 v[144:147], v160 offset:2048\n\t" \
    "s_nop 7\n\t" \
    "s_nop 1\n\t" \
    "v_mfma_f32_32x32x4_2b_bf16 v[24:55], v[148:149], v[158:159], v[24:55]\n\t" \
    "ds_read_b64 v[148:149], v161 offset:8704\n\t" \
    "ds_read_b128 v[136:139], v162 offset:10240\n\t" \
    "s_nop 7\n\t" \
    "s_nop 7\n\t" \
    "s_nop 1\n\t" \
    ".p2alignl 6, 3212836864\n\t" \
    "LOOP%=:\n\t" \
    "s_setprio 3\n\t" \
    "s_waitcnt lgkmcnt(2)\n\t" \
    "v_mfma_f32_32x32x16_bf16 v[56:71], v[144:147], v[150:153], 0\n\t" \
    "v_exp_f32 v24, v24\n\t" \
    "v_exp_f32 v25, v25\n\t" \
    "v_exp_f32 v26, v26\n\t" \
    "v_exp_f32 v27, v27\n\t" \
    "v_mfma_f32_32x32x16_bf16 v[104:119], v[140:143], v[132:135], v[104:119]\n\t" \
    "ds_read_b128 v[140:143], v162 offset:10880\n\t" \
    "v_exp_f32 v28, v28\n\t" \
    "v_exp_f32 v29, v29\n\t" \
    "v_exp_f32 v30, v30\n\t" \
    "v_exp_f32 v31, v31\n\t" \
    "v_mfma_f32_32x32x16_bf16 v[72:87], v[144:147], v[154:157], 0\n\t" \
    "ds_read_b128 v[144:147], v160 offset:4096\n\t" \
    "v_exp_f32 v40, v40\n\t" \
    "v_exp_f32 v41, v41\n\t" \
    "v_cvt_pk_bf16_f32 v120, v24, v25\n\t" \
    "v_cvt_pk_bf16_f32 v121, v26, v27\n\t" \
    "v_cvt_pk_bf16_f32 v122, v28, v29\n\t" \
    "v_cvt_pk_bf16_f32 v123, v30, v31\n\t" \
    "v_exp_f32 v42, v42\n\t" \
    "s_waitcnt lgkmcnt(2)\n\t" \
    "v_mfma_f32_32x32x16_bf16 v[88:103], v[136:139], v[120:123], v[88:103]\n\t" \
    "v_exp_f32 v43, v43\n\t" \
    "v_exp_f32 v44, v44\n\t" \
    "v_exp_f32 v45, v45\n\t" \
    "v_exp_f32 v46, v46\n\t" \
    "v_exp_f32 v47, v47\n\t" \
    "v_mfma_f32_32x32x4_2b_bf16 v[56:87], v[148:149], v[158:159], v[56:87]\n\t" \
    "ds_read_b64 v[148:149], v161 offset:9216\n\t" \
    "s_setprio 2\n\t" \
    "v_exp_f32 v32, v32\n\t" \
    "v_cvt_pk_bf16_f32 v128, v40, v41\n\t" \
    "v_cvt_pk_bf16_f32 v129, v42, v43\n\t" \
    "v_cvt_pk_bf16_f32 v130, v44, v45\n\t" \
    "v_cvt_pk_bf16_f32 v131, v46, v47\n\t" \
    "v_exp_f32 v33, v33\n\t" \
    "v_exp_f32 v34, v34\n\t" \
    "v_exp_f32 v35, v35\n\t" \
    "v_exp_f32 v36, v36\n\t" \
    "v_exp_f32 v37, v37\n\t" \
    "v_exp_f32 v38, v38\n\t" \
    "v_mfma_f32_32x32x16_bf16 v[104:119], v[136:139], v[128:131], v[104:119]\n\t" \
    "ds_read_b128 v[136:139], v162 offset:12800\n\t" \
    "v_exp_f32 v39, v39\n\t" \
    "v_exp_f32 v48, v48\n\t" \
    "v_cvt_pk_bf16_f32 v124, v32, v33\n\t" \
    "v_cvt_pk_bf16_f32 v125, v34, v35\n\t" \
    "v_cvt_pk_bf16_f32 v126, v36, v37\n\t" \
    "v_cvt_pk_bf16_f32 v127, v38, v39\n\t" \
    "v_exp_f32 v49, v49\n\t" \
    "s_waitcnt lgkmcnt(3)\n\t" \
    "v_mfma_f32_32x32x16_bf16 v[88:103], v[140:143], v[124:127], v[88:103]\n\t" \
    "v_exp_f32 v50, v50\n\t" \
    "v_exp_f32 v51, v51\n\t" \
    "v_exp_f32 v52, v52\n\t" \
    "v_exp_f32 v53, v53\n\t" \
    "v_exp_f32 v54, v54\n\t" \
    "v_exp_f32 v55, v55\n\t" \
    "v_cvt_pk_bf16_f32 v132, v48, v49\n\t" \
    "v_cvt_pk_bf16_f32 v133, v50, v51\n\t" \
    "v_cvt_pk_bf16_f32 v134, v52, v53\n\t" \
    "v_cvt_pk_bf16_f32 v135, v54, v55\n\t" \
    "s_setprio 1\n\t" \
    "s_waitcnt lgkmcnt(2)\n\t" \
    "v_mfma_f32_32x32x16_bf16 v[24:39], v[144:147], v[150:153], 0\n\t" \
    "v_exp_f32 v56, v56\n\t" \
    "v_exp_f32 v57, v57\n\t" \
    "v_exp_f32 v58, v58\n\t" \
    "v_exp_f32 v59, v59\n\t" \
    "v_mfma_f32_32x32x16_bf16 v[104:119], v[140:143], v[132:135], v[104:119]\n\t" \
    "ds_read_b128 v[140:143], v162 offset:13440\n\t" \
    "v_exp_f32 v60, v60\n\t" \
    "v_exp_f32 v61, v61\n\t" \
    "v_exp_f32 v62, v62\n\t" \
    "v_exp_f32 v63, v63\n\t" \
    "v_mfma_f32_32x32x16_bf16 v[40:55], v[144:147], v[154:157], 0\n\t" \
    "ds_read_b128 v[144:147], v160 offset:6144\n\t" \
    "v_exp_f32 v72, v72\n\t" \
    "v_exp_f32 v73, v73\n\t" \
    "v_cvt_pk_bf16_f32 v120, v56, v57\n\t" \
    "v_cvt_pk_bf16_f32 v121, v58, v59\n\t" \
    "v_cvt_pk_bf16_f32 v122, v60, v61\n\t" \
    "v_cvt_pk_bf16_f32 v123, v62, v63\n\t" \
    "v_exp_f32 v74, v74\n\t" \
    "s_waitcnt lgkmcnt(2)\n\t" \
    "v_mfma_f32_32x32x16_bf16 v[88:103], v[136:139], v[120:123], v[88:103]\n\t" \
    "v_exp_f32 v75, v75\n\t" \
    "v_exp_f32 v76, v76\n\t" \
    "v_exp_f32 v77, v77\n\t" \
    "v_exp_f32 v78, v78\n\t" \
    "v_exp_f32 v79, v79\n\t" \
    "v_mfma_f32_32x32x4_2b_bf16 v[24:55], v[148:149], v[158:159], v[24:55]\n\t" \
    "ds_read_b64 v[148:149], v161 offset:9728\n\t" \
    "s_setprio 0\n\t" \
    "v_exp_f32 v64, v64\n\t" \
    "v_cvt_pk_bf16_f32 v128, v72, v73\n\t" \
    "v_cvt_pk_bf16_f32 v129, v74, v75\n\t" \
    "v_cvt_pk_bf16_f32 v130, v76, v77\n\t" \
    "v_cvt_pk_bf16_f32 v131, v78, v79\n\t" \
    "v_exp_f32 v65, v65\n\t" \
    "v_exp_f32 v66, v66\n\t" \
    "v_exp_f32 v67, v67\n\t" \
    "v_exp_f32 v68, v68\n\t" \
    "v_exp_f32 v69, v69\n\t" \
    "v_exp_f32 v70, v70\n\t" \
    "v_mfma_f32_32x32x16_bf16 v[104:119], v[136:139], v[128:131], v[104:119]\n\t" \
    "ds_read_b128 v[136:139], v162 offset:15360\n\t" \
    "v_exp_f32 v71, v71\n\t" \
    "v_exp_f32 v80, v80\n\t" \
    "v_cvt_pk_bf16_f32 v124, v64, v65\n\t" \
    "v_cvt_pk_bf16_f32 v125, v66, v67\n\t" \
    "v_cvt_pk_bf16_f32 v126, v68, v69\n\t" \
    "v_cvt_pk_bf16_f32 v127, v70, v71\n\t" \
    "v_exp_f32 v81, v81\n\t" \
    "s_waitcnt lgkmcnt(3)\n\t" \
    "v_mfma_f32_32x32x16_bf16 v[88:103], v[140:143], v[124:127], v[88:103]\n\t" \
    "v_exp_f32 v82, v82\n\t" \
    "v_exp_f32 v83, v83\n\t" \
    "v_exp_f32 v84, v84\n\t" \
    "v_exp_f32 v85, v85\n\t" \
    "v_exp_f32 v86, v86\n\t" \
    "v_exp_f32 v87, v87\n\t" \
    "v_cvt_pk_bf16_f32 v132, v80, v81\n\t" \
    "v_cvt_pk_bf16_f32 v133, v82, v83\n\t" \
    "v_cvt_pk_bf16_f32 v134, v84, v85\n\t" \
    "v_cvt_pk_bf16_f32 v135, v86, v87\n\t" \
    "s_waitcnt vmcnt(0)\n\t" \
    "s_barrier\n\t" \
    "v_add_u32 v160, %[dK], v160\n\t" \
    "v_add_u32 v161, %[dK], v161\n\t" \
    "s_setprio 3\n\t" \
    "s_waitcnt lgkmcnt(2)\n\t" \
    "v_mfma_f32_32x32x16_bf16 v[56:71], v[144:147], v[150:153], 0\n\t" \
    "v_exp_f32 v24, v24\n\t" \
    "v_exp_f32 v25, v25\n\t" \
    "v_exp_f32 v26, v26\n\t" \
    "v_exp_f32 v27, v27\n\t" \
    "v_mfma_f32_32x32x16_bf16 v[104:119], v[140:143], v[132:135], v[104:119]\n\t" \
    "ds_read_b128 v[140:143], v162 offset:16000\n\t" \
    "v_exp_f32 v28, v28\n\t" \
    "v_exp_f32 v29, v29\n\t" \
    "v_exp_f32 v30, v30\n\t" \
    "v_exp_f32 v31, v31\n\t" \
    "v_mfma_f32_32x32x16_bf16 v[72:87], v[144:147], v[154:157], 0\n\t" \
    "ds_read_b128 v[144:147], v160 offset:0\n\t" \
    "v_exp_f32 v40, v40\n\t" \
    "v_exp_f32 v41, v41\n\t" \
    "v_cvt_pk_bf16_f32 v120, v24, v25\n\t" \
    "v_cvt_pk_bf16_f32 v121, v26, v27\n\t" \
    "v_cvt_pk_bf16_f32 v122, v28, v29\n\t" \
    "v_cvt_pk_bf16_f32 v123, v30, v31\n\t" \
    "v_exp_f32 v42, v42\n\t" \
    "s_waitcnt lgkmcnt(2)\n\t" \
    "v_mfma_f32_32x32x16_bf16 v[88:103], v[136:139], v[120:123], v[88:103]\n\t" \
    "v_exp_f32 v43, v43\n\t" \
    "v_exp_f32 v44, v44\n\t" \
    "v_exp_f32 v45, v45\n\t" \
    "v_exp_f32 v46, v46\n\t" \
    "v_exp_f32 v47, v47\n\t" \
    "v_mfma_f32_32x32x4_2b_bf16 v[56:87], v[148:149], v[158:159], v[56:87]\n\t" \
    "ds_read_b64 v[148:149], v161 offset:8192\n\t" \
    "s_setprio 2\n\t" \
    "v_exp_f32 v32, v32\n\t" \
    "v_cvt_pk_bf16_f32 v128, v40, v41\n\t" \
    "v_cvt_pk_bf16_f32 v129, v42, v43\n\t" \
    "v_cvt_pk_bf16_f32 v130, v44, v45\n\t" \
    "v_cvt_pk_bf16_f32 v131, v46, v47\n\t" \
    "v_exp_f32 v33, v33\n\t" \
    "v_exp_f32 v34, v34\n\t" \
    "v_exp_f32 v35, v35\n\t" \
    "v_exp_f32 v36, v36\n\t" \
    "v_exp_f32 v37, v37\n\t" \
    "v_exp_f32 v38, v38\n\t" \
    "v_mfma_f32_32x32x16_bf16 v[104:119], v[136:139], v[128:131], v[104:119]\n\t" \
    "ds_read_b128 v[136:139], v162 offset:17920\n\t" \
    "v_exp_f32 v39, v39\n\t" \
    "v_exp_f32 v48, v48\n\t" \
    "v_cvt_pk_bf16_f32 v124, v32, v33\n\t" \
    "v_cvt_pk_bf16_f32 v125, v34, v35\n\t" \
    "v_cvt_pk_bf16_f32 v126, v36, v37\n\t" \
    "v_cvt_pk_bf16_f32 v127, v38, v39\n\t" \
    "v_exp_f32 v49, v49\n\t" \
    "s_waitcnt lgkmcnt(3)\n\t" \
    "v_mfma_f32_32x32x16_bf16 v[88:103], v[140:143], v[124:127], v[88:103]\n\t" \
    "v_exp_f32 v50, v50\n\t" \
    "v_exp_f32 v51, v51\n\t" \
    "v_exp_f32 v52, v52\n\t" \
    "v_exp_f32 v53, v53\n\t" \
    "v_exp_f32 v54, v54\n\t" \
    "v_exp_f32 v55, v55\n\t" \
    "v_cvt_pk_bf16_f32 v132, v48, v49\n\t" \
    "v_cvt_pk_bf16_f32 v133, v50, v51\n\t" \
    "v_cvt_pk_bf16_f32 v134, v52, v53\n\t" \
    "v_cvt_pk_bf16_f32 v135, v54, v55\n\t" \
    "s_setprio 1\n\t" \
    "s_waitcnt lgkmcnt(2)\n\t" \
    "v_mfma_f32_32x32x16_bf16 v[24:39], v[144:147], v[150:153], 0\n\t" \
    "v_exp_f32 v56, v56\n\t" \
    "v_exp_f32 v57, v57\n\t" \
    "v_exp_f32 v58, v58\n\t" \
    "v_exp_f32 v59, v59\n\t" \
    "v_mfma_f32_32x32x16_bf16 v[104:119], v[140:143], v[132:135], v[104:119]\n\t" \
    "ds_read_b128 v[140:143], v162 offset:18560\n\t" \
    "v_exp_f32 v60, v60\n\t" \
    "v_exp_f32 v61, v61\n\t" \
    "v_exp_f32 v62, v62\n\t" \
    "v_exp_f32 v63, v63\n\t" \
    "v_mfma_f32_32x32x16_bf16 v[40:55], v[144:147], v[154:157], 0\n\t" \
    "ds_read_b128 v[144:147], v160 offset:2048\n\t" \
    "v_exp_f32 v72, v72\n\t" \
    "v_exp_f32 v73, v73\n\t" \
    "v_cvt_pk_bf16_f32 v120, v56, v57\n\t" \
    "v_cvt_pk_bf16_f32 v121, v58, v59\n\t" \
    "v_cvt_pk_bf16_f32 v122, v60, v61\n\t" \
    "v_cvt_pk_bf16_f32 v123, v62, v63\n\t" \
    "v_exp_f32 v74, v74\n\t" \
    "s_waitcnt lgkmcnt(2)\n\t" \
    "v_mfma_f32_32x32x16_bf16 v[88:103], v[136:139], v[120:123], v[88:103]\n\t" \
    "v_exp_f32 v75, v75\n\t" \
    "v_exp_f32 v76, v76\n\t" \
    "v_exp_f32 v77, v77\n\t" \
    "v_exp_f32 v78, v78\n\t" \
    "v_exp_f32 v79, v79\n\t" \
    "v_mfma_f32_32x32x4_2b_bf16 v[24:55], v[148:149], v[158:159], v[24:55]\n\t" \
    "ds_read_b64 v[148:149], v161 offset:8704\n\t" \
    "s_setprio 0\n\t" \
    "v_exp_f32 v64, v64\n\t" \
    "v_cvt_pk_bf16_f32 v128, v72, v73\n\t" \
    "v_cvt_pk_bf16_f32 v129, v74, v75\n\t" \
    "v_cvt_pk_bf16_f32 v130, v76, v77\n\t" \
    "v_cvt_pk_bf16_f32 v131, v78, v79\n\t" \
    "v_exp_f32 v65, v65\n\t" \
    "v_exp_f32 v66, v66\n\t" \
    "v_exp_f32 v67, v67\n\t" \
    "v_exp_f32 v68, v68\n\t" \
    "v_exp_f32 v69, v69\n\t" \
    "v_exp_f32 v70, v70\n\t" \
    "v_mfma_f32_32x32x16_bf16 v[104:119], v[136:139], v[128:131], v[104:119]\n\t" \
    "v_add_u32 v162, %[dK], v162\n\t" \
    "ds_read_b128 v[136:139], v162 offset:10240\n\t" \
    "v_exp_f32 v71, v71\n\t" \
    "v_exp_f32 v80, v80\n\t" \
    "v_cvt_pk_bf16_f32 v124, v64, v65\n\t" \
    "v_cvt_pk_bf16_f32 v125, v66, v67\n\t" \
    "v_cvt_pk_bf16_f32 v126, v68, v69\n\t" \
    "v_cvt_pk_bf16_f32 v127, v70, v71\n\t" \
    "v_exp_f32 v81, v81\n\t" \
    "s_waitcnt lgkmcnt(3)\n\t" \
    "v_mfma_f32_32x32x16_bf16 v[88:103], v[140:143], v[124:127], v[88:103]\n\t" \
    "v_exp_f32 v82, v82\n\t" \
    "v_exp_f32 v83, v83\n\t" \
    "v_exp_f32 v84, v84\n\t" \
    "v_exp_f32 v85, v85\n\t" \
    "v_exp_f32 v86, v86\n\t" \
    "v_exp_f32 v87, v87\n\t" \
    "v_cvt_pk_bf16_f32 v132, v80, v81\n\t" \
    "v_cvt_pk_bf16_f32 v133, v82, v83\n\t" \
    "v_cvt_pk_bf16_f32 v134, v84, v85\n\t" \
    "v_cvt_pk_bf16_f32 v135, v86, v87\n\t" \
    "s_add_u32 %[slot], %[slot], 1\n\t" \
    "s_mov_b32 %[dK], 20480\n\t" \
    "s_cmp_eq_u32 %[slot], 2\n\t" \
    "s_cselect_b32 %[dK], 4294926336, %[dK]\n\t" \
    "s_cmp_eq_u32 %[slot], 3\n\t" \
    "s_cselect_b32 %[slot], 0, %[slot]\n\t" \
    "s_sub_u32 %[cnt], %[cnt], 1\n\t" \
    "s_cmp_lg_u32 %[cnt], 0\n\t" \
    "s_cbranch_scc1 LOOP%=\n\t" \
    "s_waitcnt lgkmcnt(0)\n\t" \
    "s_setprio 0\n\t" \
    "v_mfma_f32_32x32x16_bf16 v[104:119], v[140:143], v[132:135], v[104:119]\n\t" \
    "s_nop 7\n\t" \
    "s_nop 7\n\t" \
    ""
#define ATTN_CLOB_REGS "v24", "v25", "v26", "v27", "v28", "v29", "v30", "v31", "v32", "v33", "v34", "v35", "v36", "v37", "v38", "v39", "v40", "v41", "v42", "v43", "v44", "v45", "v46", "v47", "v48", "v49", "v50", "v51", "v52", "v53", "v54", "v55", "v56", "v57", "v58", "v59", "v60", "v61", "v62", "v63", "v64", "v65", "v66", "v67", "v68", "v69", "v70", "v71", "v72", "v73", "v74", "v75", "v76", "v77", "v78", "v79", "v80", "v81", "v82", "v83", "v84", "v85", "v86", "v87", "v120", "v121", "v122", "v123", "v124", "v125", "v126", "v127", "v128", "v129", "v130", "v131", "v132", "v133", "v134", "v135", "v136", "v137", "v138", "v139", "v140", "v141", "v142", "v143", "v144", "v145", "v146", "v147", "v148", "v149"

#define CONV_PAIR_ASM \
    "ds_read_b64_tr_b16 v[56:57], v104 offset:32\n\t" \
    "ds_read_b64_tr_b16 v[58:59], v104 offset:0\n\t" \
    "ds_read_b64_tr_b16 v[60:61], v104 offset:160\n\t" \
    "ds_read_b64_tr_b16 v[62:63], v104 offset:128\n\t" \
    "ds_read_b64_tr_b16 v[64:65], v104 offset:288\n\t" \
    "ds_read_b64_tr_b16 v[66:67], v104 offset:256\n\t" \
    "v_subrev_u32 v105, 192, v105\n\t" \
    "ds_read_b128 v[72:75], v105 offset:192\n\t" \
    "ds_read_b128 v[76:79], v105 offset:160\n\t" \
    "ds_read_b128 v[80:83], v105 offset:128\n\t" \
    "ds_read_b128 v[84:87], v105 offset:96\n\t" \
    "ds_read_b128 v[88:91], v105 offset:64\n\t" \
    "ds_read_b128 v[92:95], v105 offset:32\n\t" \
    "ds_read_b128 v[96:99], v105 offset:0\n\t" \
    "v_mov_b32 v24, 0\n\t" \
    "v_mov_b32 v25, 0\n\t" \
    "v_mov_b32 v26, 0\n\t" \
    "v_mov_b32 v27, 0\n\t" \
    "v_mov_b32 v28, 0\n\t" \
    "v_mov_b32 v29, 0\n\t" \
    "v_mov_b32 v30, 0\n\t" \
    "v_mov_b32 v31, 0\n\t" \
    "v_mov_b32 v32, 0\n\t" \
    "v_mov_b32 v33, 0\n\t" \
    "v_mov_b32 v34, 0\n\t" \
    "v_mov_b32 v35, 0\n\t" \
    "v_mov_b32 v36, 0\n\t" \
    "v_mov_b32 v37, 0\n\t" \
    "v_mov_b32 v38, 0\n\t" \
    "v_mov_b32 v39, 0\n\t" \
    "v_mov_b32 v40, 0\n\t" \
    "v_mov_b32 v41, 0\n\t" \
    "v_mov_b32 v42, 0\n\t" \
    "v_mov_b32 v43, 0\n\t" \
    "v_mov_b32 v44, 0\n\t" \
    "v_mov_b32 v45, 0\n\t" \
    "v_mov_b32 v46, 0\n\t" \
    "v_mov_b32 v47, 0\n\t" \
    "v_mov_b32 v48, 0\n\t" \
    "v_mov_b32 v49, 0\n\t" \
    "v_mov_b32 v50, 0\n\t" \
    "v_mov_b32 v51, 0\n\t" \
    "v_mov_b32 v52, 0\n\t" \
    "v_mov_b32 v53, 0\n\t" \
    "v_mov_b32 v54, 0\n\t" \
    "v_mov_b32 v55, 0\n\t" \
    "v_subrev_u32 v105, 256, v105\n\t" \
    "s_waitcnt lgkmcnt(0)\n\t" \
    "s_cmp_eq_u32 %[k], 0\n\t" \
    "s_cbranch_scc1 CLAST%=\n\t" \
    ".p2alignl 6, 3212836864\n\t" \
    "CLOOP%=:\n\t" \
    "s_waitcnt lgkmcnt(6)\n\t" \
    "v_mfma_f32_32x32x16_bf16 v[40:55], v[56:59], v[72:75], v[40:55]\n\t" \
    "ds_read_b64_tr_b16 v[68:69], v104 offset:416\n\t" \
    "ds_read_b64_tr_b16 v[70:71], v104 offset:384\n\t" \
    "ds_read_b128 v[100:103], v105 offset:224\n\t" \
    "v_mfma_f32_32x32x16_bf16 v[24:39], v[56:59], v[88:91], v[24:39]\n\t" \
    "s_waitcnt lgkmcnt(6)\n\t" \
    "v_mfma_f32_32x32x16_bf16 v[40:55], v[60:63], v[76:79], v[40:55]\n\t" \
    "ds_read_b64_tr_b16 v[56:57], v104 offset:544\n\t" \
    "ds_read_b64_tr_b16 v[58:59], v104 offset:512\n\t" \
    "ds_read_b128 v[72:75], v105 offset:192\n\t" \
    "v_mfma_f32_32x32x16_bf16 v[24:39], v[60:63], v[92:95], v[24:39]\n\t" \
    "s_waitcnt lgkmcnt(6)\n\t" \
    "v_mfma_f32_32x32x16_bf16 v[40:55], v[64:67], v[80:83], v[40:55]\n\t" \
    "ds_read_b64_tr_b16 v[60:61], v104 offset:672\n\t" \
    "ds_read_b64_tr_b16 v[62:63], v104 offset:640\n\t" \
    "ds_read_b128 v[76:79], v105 offset:160\n\t" \
    "v_mfma_f32_32x32x16_bf16 v[24:39], v[64:67], v[96:99], v[24:39]\n\t" \
    "s_waitcnt lgkmcnt(6)\n\t" \
    "v_mfma_f32_32x32x16_bf16 v[40:55], v[68:71], v[84:87], v[40:55]\n\t" \
    "ds_read_b64_tr_b16 v[64:65], v104 offset:800\n\t" \
    "ds_read_b64_tr_b16 v[66:67], v104 offset:768\n\t" \
    "ds_read_b128 v[80:83], v105 offset:128\n\t" \
    "v_mfma_f32_32x32x16_bf16 v[24:39], v[68:71], v[100:103], v[24:39]\n\t" \
    "s_waitcnt lgkmcnt(6)\n\t" \
    "v_mfma_f32_32x32x16_bf16 v[40:55], v[56:59], v[88:91], v[40:55]\n\t" \
    "ds_read_b64_tr_b16 v[68:69], v104 offset:928\n\t" \
    "ds_read_b64_tr_b16 v[70:71], v104 offset:896\n\t" \
    "ds_read_b128 v[84:87], v105 offset:96\n\t" \
    "v_mfma_f32_32x32x16_bf16 v[24:39], v[56:59], v[72:75], v[24:39]\n\t" \
    "s_waitcnt lgkmcnt(6)\n\t" \
    "v_mfma_f32_32x32x16_bf16 v[40:55], v[60:63], v[92:95], v[40:55]\n\t" \
    "ds_read_b64_tr_b16 v[56:57], v104 offset:1056\n\t" \
    "ds_read_b64_tr_b16 v[58:59], v104 offset:1024\n\t" \
    "ds_read_b128 v[88:91], v105 offset:64\n\t" \
    "v_mfma_f32_32x32x16_bf16 v[24:39], v[60:63], v[76:79], v[24:39]\n\t" \
    "s_waitcnt lgkmcnt(6)\n\t" \
    "v_mfma_f32_32x32x16_bf16 v[40:55], v[64:67], v[96:99], v[40:55]\n\t" \
    "ds_read_b64_tr_b16 v[60:61], v104 offset:1184\n\t" \
    "ds_read_b64_tr_b16 v[62:63], v104 offset:1152\n\t" \
    "ds_read_b128 v[92:95], v105 offset:32\n\t" \
    "v_mfma_f32_32x32x16_bf16 v[24:39], v[64:67], v[80:83], v[24:39]\n\t" \
    "s_waitcnt lgkmcnt(6)\n\t" \
    "v_mfma_f32_32x32x16_bf16 v[40:55], v[68:71], v[100:103], v[40:55]\n\t" \
    "ds_read_b64_tr_b16 v[64:65], v104 offset:1312\n\t" \
    "ds_read_b64_tr_b16 v[66:67], v104 offset:1280\n\t" \
    "ds_read_b128 v[96:99], v105 offset:0\n\t" \
    "v_mfma_f32_32x32x16_bf16 v[24:39], v[68:71], v[84:87], v[24:39]\n\t" \
    "v_add_u32 v104, 0x400, v104\n\t" \
    "v_subrev_u32 v105, 0x100, v105\n\t" \
    "s_sub_u32 %[k], %[k], 1\n\t" \
    "s_cmp_lg_u32 %[k], 0\n\t" \
    "s_cbranch_scc1 CLOOP%=\n\t" \
    "CLAST%=:\n\t" \
    "s_waitcnt lgkmcnt(6)\n\t" \
    "v_mfma_f32_32x32x16_bf16 v[40:55], v[56:59], v[72:75], v[40:55]\n\t" \
    "ds_read_b64_tr_b16 v[68:69], v104 offset:416\n\t" \
    "ds_read_b64_tr_b16 v[70:71], v104 offset:384\n\t" \
    "ds_read_b128 v[100:103], v105 offset:224\n\t" \
    "v_mfma_f32_32x32x16_bf16 v[24:39], v[56:59], v[88:91], v[24:39]\n\t" \
    "s_waitcnt lgkmcnt(6)\n\t" \
    "v_mfma_f32_32x32x16_bf16 v[40:55], v[60:63], v[76:79], v[40:55]\n\t" \
    "ds_read_b64_tr_b16 v[56:57], v104 offset:544\n\t" \
    "ds_read_b64_tr_b16 v[58:59], v104 offset:512\n\t" \
    "ds_read_b128 v[72:75], v105 offset:192\n\t" \
    "v_mfma_f32_32x32x16_bf16 v[24:39], v[60:63], v[92:95], v[24:39]\n\t" \
    "s_waitcnt lgkmcnt(6)\n\t" \
    "v_mfma_f32_32x32x16_bf16 v[40:55], v[64:67], v[80:83], v[40:55]\n\t" \
    "ds_read_b64_tr_b16 v[60:61], v104 offset:672\n\t" \
    "ds_read_b64_tr_b16 v[62:63], v104 offset:640\n\t" \
    "ds_read_b128 v[76:79], v105 offset:160\n\t" \
    "v_mfma_f32_32x32x16_bf16 v[24:39], v[64:67], v[96:99], v[24:39]\n\t" \
    "s_waitcnt lgkmcnt(6)\n\t" \
    "v_mfma_f32_32x32x16_bf16 v[40:55], v[68:71], v[84:87], v[40:55]\n\t" \
    "ds_read_b64_tr_b16 v[64:65], v104 offset:800\n\t" \
    "ds_read_b64_tr_b16 v[66:67], v104 offset:768\n\t" \
    "ds_read_b128 v[80:83], v105 offset:128\n\t" \
    "v_mfma_f32_32x32x16_bf16 v[24:39], v[68:71], v[100:103], v[24:39]\n\t" \
    "s_waitcnt lgkmcnt(6)\n\t" \
    "v_mfma_f32_32x32x16_bf16 v[40:55], v[56:59], v[88:91], v[40:55]\n\t" \
    "ds_read_b64_tr_b16 v[68:69], v104 offset:928\n\t" \
    "ds_read_b64_tr_b16 v[70:71], v104 offset:896\n\t" \
    "ds_read_b128 v[84:87], v105 offset:96\n\t" \
    "v_mfma_f32_32x32x16_bf16 v[24:39], v[56:59], v[72:75], v[24:39]\n\t" \
    "s_waitcnt lgkmcnt(6)\n\t" \
    "v_mfma_f32_32x32x16_bf16 v[40:55], v[60:63], v[92:95], v[40:55]\n\t" \
    "ds_read_b64_tr_b16 v[56:57], v104 offset:1056\n\t" \
    "ds_read_b64_tr_b16 v[58:59], v104 offset:1024\n\t" \
    "ds_read_b128 v[88:91], v105 offset:64\n\t" \
    "s_waitcnt lgkmcnt(6)\n\t" \
    "v_mfma_f32_32x32x16_bf16 v[40:55], v[64:67], v[96:99], v[40:55]\n\t" \
    "ds_read_b64_tr_b16 v[60:61], v104 offset:1184\n\t" \
    "ds_read_b64_tr_b16 v[62:63], v104 offset:1152\n\t" \
    "ds_read_b128 v[92:95], v105 offset:32\n\t" \
    "s_waitcnt lgkmcnt(6)\n\t" \
    "v_mfma_f32_32x32x16_bf16 v[40:55], v[68:71], v[100:103], v[40:55]\n\t" \
    "ds_read_b64_tr_b16 v[64:65], v104 offset:1312\n\t" \
    "ds_read_b64_tr_b16 v[66:67], v104 offset:1280\n\t" \
    "ds_read_b128 v[96:99], v105 offset:0\n\t" \
    "v_add_u32 v104, 0x400, v104\n\t" \
    "v_subrev_u32 v105, 0x100, v105\n\t" \
    "s_waitcnt lgkmcnt(6)\n\t" \
    "v_mfma_f32_32x32x16_bf16 v[40:55], v[56:59], v[72:75], v[40:55]\n\t" \
    "s_waitcnt lgkmcnt(0)\n\t" \
    "s_nop 7\n\t" \
    "s_nop 7\n\t" \
    ""
#define CONV_CLOB_REGS "v56", "v57", "v58", "v59", "v60", "v61", "v62", "v63", "v64", "v65", "v66", "v67", "v68", "v69", "v70", "v71", "v72", "v73", "v74", "v75", "v76", "v77", "v78", "v79", "v80", "v81", "v82", "v83", "v84", "v85", "v86", "v87", "v88", "v89", "v90", "v91", "v92", "v93", "v94", "v95", "v96", "v97", "v98", "v99", "v100", "v101", "v102", "v103"

#define ATTN_CLOB "memory", "scc", ATTN_CLOB_REGS

__device__ __forceinline__ f32x16 attn_exact(rsrc_t rs, int h, u32x4 q1, u32x2 q2, int lane) {
    const int hh = lane >> 5, r = lane & 31, dv = r < 19 ? r : 19;
    f32x16 o = {}; float m = -INFINITY;
#pragma unroll 1
    for (int kt = 0; kt < 64; ++kt) {
        const unsigned c = WS_KV + h * KV_HEAD + (kt >> 3) * KV_CHUNK; const int t = kt & 7;
        const u32x4 ka = ld16(rs, t * 1024 + lane * 16, c);
        u32x2 k2 = {ld4(rs, KV_K2 + (t * 32 + r) * 8, c), 0u};
        if (hh) k2[0] = 0u;
        const u32x4 v0 = ld16(rs, KV_V + (2 * t) * 640 + hh * 320 + dv * 16, c), v1 = ld16(rs, KV_V + (2 * t + 1) * 640 + hh * 320 + dv * 16, c);
        f32x16 s = {};
        s = mfma32(ka, q1, s); s = mfma32k8(k2, q2, s);
        float tm = s[0];
#pragma unroll
        for (int i = 1; i < 16; ++i) tm = fmaxf(tm, s[i]);
        const auto sw = __builtin_amdgcn_permlane32_swap(__float_as_uint(tm), __float_as_uint(tm), false, false);
        tm = fmaxf(__uint_as_float(sw[0]), __uint_as_float(sw[1]));
        const float mn = fmaxf(m, tm), al = __builtin_amdgcn_exp2f(m - mn);
        m = mn;
#pragma unroll
        for (int i = 0; i < 16; ++i) { o[i] *= al; s[i] = __builtin_amdgcn_exp2f(s[i] - mn); }
        const u32x4 p0 = {cvtpk(s[0], s[1]), cvtpk(s[2], s[3]), cvtpk(s[4], s[5]), cvtpk(s[6], s[7])};
        const u32x4 p1 = {cvtpk(s[8], s[9]), cvtpk(s[10], s[11]), cvtpk(s[12], s[13]), cvtpk(s[14], s[15])};
        o = mfma32(v0, p0, o); o = mfma32(v1, p1, o);
    }
    return o;
}

__device__ __forceinline__ lds_ptr xch_slot(lds_ptr lds, int w) { return lds + L_RING + (w < 6 ? w * 3072 : 2 * KV_CHUNK + (w - 6) * 3072); }

__device__ __forceinline__ void attn_wave(const Args& a, rsrc_t rs, lds_ptr lds, int h, int qt, int w, int lane, u32x8 q1, u32x2 q2, u64 tag, char* dbg_ws) {
    const int qg = w & 3, kh = w >> 2;
    int dv = lane & 31; dv = dv < 19 ? dv : 19;
    const unsigned lds0 = (unsigned)(uintptr_t)lds + L_RING;
    unsigned ak1 = lds0 + lane * 16 + kh * 1024, ak2 = lds0 + (lane & 31) * 8 + kh * 256, av = lds0 + (lane >> 5) * 320 + dv * 16 + kh * 1280;
    unsigned slot = 0, dK = KV_CHUNK, cnt = 8;
    f32x32 o;
    STAMP(6);
    asm volatile(ATTN_LOOP_ASM
        : "={v[88:119]}"(o), "+{v160}"(ak1), "+{v161}"(ak2), "+{v162}"(av), [slot] "+s"(slot), [dK] "+s"(dK), [cnt] "+s"(cnt)
        : "{v[150:157]}"(q1), "{v[158:159]}"(q2)
        : ATTN_CLOB);
    lane = lane_id();
    const int hh = lane >> 5;
    STAMP(7);
    {
        lds_ptr xw = xch_slot(lds, w) + lane * 16;
#pragma unroll
        for (int i = 0; i < 3; ++i)
            *(LDS_AS f32x4*)(xw + i * 1024) = kh ? f32x4{o[4 * i], o[4 * i + 1], o[4 * i + 2], o[4 * i + 3]} : f32x4{o[16 + 4 * i], o[17 + 4 * i], o[18 + 4 * i], o[19 + 4 * i]};
    }
    BARRIER();
    STAMP(28);
    float v[12];
    {
        lds_cptr xr = (lds_cptr)xch_slot(lds, w ^ 4) + lane * 16;
#pragma unroll
        for (int i = 0; i < 3; ++i) {
            const f32x4 t = *(const LDS_AS f32x4*)(xr + i * 1024);
#pragma unroll
            for (int j = 0; j < 4; ++j) v[4 * i + j] = t[j] + (kh ? o[16 + 4 * i + j] : o[4 * i + j]);
        }
    }
    auto lsw = __builtin_amdgcn_permlane32_swap(__float_as_uint(v[10]), __float_as_uint(v[10]), false, false);
    float l = __uint_as_float(lsw[0]);
    bool ok = l >= 0x1p-40f && l <= 0x1p64f;
#pragma unroll
    for (int i = 0; i < 10; ++i) ok = ok && (fabsf(v[i]) <= 0x1p126f || (hh && i >= 8));
    if (__builtin_expect(!__all(ok), 0)) {
        const auto qs = __builtin_amdgcn_permlane32_swap(q2[0], q2[0], false, false);
        const u32x2 q2e = {hh ? 0u : (kh ? qs[1] : qs[0]), 0u};
        const u32x4 q1e = kh ? u32x4{q1[4], q1[5], q1[6], q1[7]} : u32x4{q1[0], q1[1], q1[2], q1[3]};
        const f32x16 e = attn_exact(rs, h, q1e, q2e, lane);
#pragma unroll
        for (int i = 0; i < 12; ++i) v[i] = e[i];
        lsw = __builtin_amdgcn_permlane32_swap(__float_as_uint(v[10]), __float_as_uint(v[10]), false, false);
        l = __uint_as_float(lsw[0]);
    }
    STAMP(29);
    const float inv = 1.0f / l;
    const int t = qt * 256 + qg * 64 + 32 * kh + (lane & 31);
    st16(rs, t * 32 + hh * 16, WS_O1 + h * 65536,
         u32x4{cvtpk(v[0] * inv, v[1] * inv), cvtpk(v[2] * inv, v[3] * inv), cvtpk(v[4] * inv, v[5] * inv), cvtpk(v[6] * inv, v[7] * inv)});
    if (hh == 0) st4(rs, t * 4, WS_O2 + h * 8192, cvtpk(v[8] * inv, v[9] * inv));
    STAMP(30);
    publish(a.ws, lds, 1, 8, FL_F2 + qt * 256 + h * 8, tag, lane);
}

__device__ __forceinline__ int conv_channel(int h, int qt, int pid, int j) { return j < 2 ? 18 * h + 2 * qt + j : 18 * (pid >> 1) + 16 + (pid & 1); }

struct ConvPtr { lds_cptr ka, ub; };
__device__ __forceinline__ ConvPtr conv_ptr(lds_ptr lds, int j, int t, int lane) {
    const int g = lane >> 4, q = (lane & 15) >> 2, p = lane & 3, hh = lane >> 5, s = (lane & 31) >> 3, n1 = lane & 7;
    return ConvPtr{(lds_cptr)lds + l_ck(j) + 2 * (64 + 16 * (g & 1) - 4 * (8 * hh + q) + 4 * p - 16),
                   (lds_cptr)lds + l_cu(j) + s * 1152 + 2 * (64 + 8 * (8 * t + n1) + 8 * hh) - 96};
}
struct ConvFrag { u32x4 a[4], b[4]; };
__device__ __forceinline__ void conv_fetch(ConvFrag& f, ConvPtr& c) {
#pragma unroll
    for (int i = 0; i < 4; ++i) {
        const u32x2 lo = lds_tr(c.ka + 128 * i + 32), hi = lds_tr(c.ka + 128 * i);
        f.a[i] = u32x4{lo[0], lo[1], hi[0], hi[1]}; f.b[i] = lds_rd128(c.ub + 96 - 32 * i);
    }
    c.ka += 512; c.ub -= 128;
}
__device__ __forceinline__ void conv_mma(f32x16& acc, const ConvFrag& f) {
#pragma unroll
    for (int i = 0; i < 4; ++i) acc = mfma32(f.a[i], f.b[i], acc);
}
template <bool LAST>
__device__ __forceinline__ void conv_run(f32x16& acc, ConvPtr& c, int nb) {
    ConvFrag f0, f1;
    conv_fetch(f0, c);
#pragma unroll 1
    while (true) {
        if (LAST || nb > 1) conv_fetch(f1, c);
        conv_mma(acc, f0);
        if (--nb == 0) { if (LAST) acc = mfma32(f1.a[0], f1.b[0], acc); break; }
        if (LAST || nb > 1) conv_fetch(f0, c);
        conv_mma(acc, f1);
        if (--nb == 0) { if (LAST) acc = mfma32(f0.a[0], f0.b[0], acc); break; }
    }
}
__device__ __forceinline__ f32x4 conv_close(const f32x16& acc, lds_ptr lds, lds_ptr scr, int j, int t, int lane) {
    const int hh = lane >> 5, s = (lane & 31) >> 3, n1 = lane & 7;
#pragma unroll
    for (int i = 0; i < 4; ++i)
        *(LDS_AS f32x4*)(scr + s * 1152 + n1 * 144 + (8 * i + 4 * hh) * 4) = f32x4{acc[4 * i], acc[4 * i + 1], acc[4 * i + 2], acc[4 * i + 3]};
    lds_cptr rc = (lds_cptr)scr + (lane >> 3) * 144 + (lane & 7) * 16;
    lds_cptr rp = lane == 0 ? rc : (lane & 7) ? rc - 16 : rc - 32;
    f32x4 y = *(const LDS_AS f32x4*)rc, cu[4], pv[4];
#pragma unroll
    for (int i = 1; i < 4; ++i) { cu[i] = *(const LDS_AS f32x4*)(rc + i * 1152); pv[i] = *(const LDS_AS f32x4*)(rp + i * 1152); if (lane == 0) pv[i] = f32x4{0.f, 0.f, 0.f, 0.f}; }
    y[0] += pv[1][3]; y[1] += cu[1][0]; y[2] += cu[1][1]; y[3] += cu[1][2];
    y[0] += pv[2][2]; y[1] += pv[2][3]; y[2] += cu[2][0]; y[3] += cu[2][1];
    y[0] += pv[3][1]; y[1] += pv[3][2]; y[2] += pv[3][3]; y[3] += cu[3][0];
    if (lane == 63) *(LDS_AS f32x4*)(lds + L_SPILL + j * 128 + t * 16) = f32x4{cu[1][3] + cu[2][2] + cu[3][1], cu[2][3] + cu[3][2], cu[3][3], 0.f};
    return y;
}
__device__ __forceinline__ f32x4 conv_tile(lds_ptr lds, lds_ptr scr, int j, int t, int lane) {
    ConvPtr c = conv_ptr(lds, j, t, lane);
    f32x16 acc = {};
    conv_run<true>(acc, c, t + 1);
    return conv_close(acc, lds, scr, j, t, lane);
}
__device__ __forceinline__ void conv_pair(lds_ptr lds, int j, int t, int lane, f32x16& accT, f32x16& accU) {
    const int g = lane >> 4, q = (lane & 15) >> 2, p = lane & 3, hh = lane >> 5, s = (lane & 31) >> 3, n1 = lane & 7;
    const unsigned lds0 = (unsigned)(uintptr_t)lds;
    unsigned ka = lds0 + l_ck(j) + 2 * (64 + 16 * (g & 1) - 4 * (8 * hh + q) + 4 * p - 16);
    unsigned ub = lds0 + l_cu(j) + s * 1152 + 128 + 16 * n1 + 16 * hh + 32 * (4 * t + 4);
    unsigned k = t >> 1;
    asm volatile(CONV_PAIR_ASM : "={v[24:39]}"(accT), "={v[40:55]}"(accU), "+{v104}"(ka), "+{v105}"(ub), [k] "+s"(k) :: "memory", "scc", CONV_CLOB_REGS);
}
__device__ __forceinline__ void conv_finish(rsrc_t rs, lds_ptr lds, int j, int d, float dskip, int t, f32x4 y, int lane) {
    if (t > 0 && lane == 0) { const f32x4 sp = *(const LDS_AS f32x4*)((lds_cptr)lds + L_SPILL + j * 128 + (t - 1) * 16); y[0] += sp[0]; y[1] += sp[1]; y[2] += sp[2]; }
    float v[4];
#pragma unroll
    for (int e = 0; e < 4; ++e) {
        const float uu = bf2f(*(const LDS_AS unsigned short*)((lds_cptr)lds + l_cu(j) + e * 1152 + 128 + (64 * t + lane) * 2));
        v[e] = bfround(y[e]) + bfround(uu * dskip);
    }
    st8(rs, (256 * t + 4 * lane) * 2, WS_AT + d * 4096, u32x2{cvtpk(v[0], v[1]), cvtpk(v[2], v[3])});
}

__device__ __forceinline__ int wo_row(int kp) {
    if (kp >= 512) return 18 * ((kp - 512) >> 1) + 16 + (kp & 1);
    const int sl = kp & 15;
    return 18 * (kp >> 4) + ((sl & 3) | ((sl & 4) << 1) | ((sl & 8) >> 1));
}

__device__ __forceinline__ void helper_wave(const Args& a, rsrc_t rs, lds_ptr lds, int h, int qt, int hw, int lane, unsigned pflag, u64 tag, char* dbg_ws) {
    char* ws = a.ws;
    auto chunk_ld = [&](u32x4 (&v)[5], int i) {
        const unsigned src = WS_KV + h * KV_HEAD + i * KV_CHUNK + hw * 5120;
#pragma unroll
        for (int e = 0; e < 5; ++e) v[e] = ld16(rs, e * 1024 + lane * 16, src);
    };
    auto chunk_st = [&](const u32x4 (&v)[5], int i) {
#pragma unroll
        for (int e = 0; e < 5; ++e) lds_wr128(lds + L_RING + (i % 3) * KV_CHUNK + hw * 5120 + e * 1024 + lane * 16, v[e]);
    };
    if (pflag) publish(ws, lds, 0, 4, pflag, tag, lane);
    STAMP(4);
    if (hw == 0) {
        flag_wait(ws, FL_FP + (lane < 32 ? (4 + 4 * (lane >> 4) + (h >> 3)) * 128 + (lane & 15) * 8 : (h >> 3) * 128 + (2 * qt + (lane & 1)) * 8),
                  lane < 34 && (lane >= 32 || (lane & 15) < 6), tag);
        lds_arrive(lds, 4, lane);
    } else lds_await(lds, 4, 1);
    STAMP(5);
#define DBG_HPRIO 3
    __builtin_amdgcn_s_setprio(DBG_HPRIO);
    u32x4 cv[5], cv1[5], cv2[5];
    chunk_ld(cv, 0); chunk_ld(cv1, 1); chunk_ld(cv2, 2);
    chunk_st(cv, 0);
    BARRIER();
    chunk_st(cv1, 1);
    BARRIER();
    if (hw == 0 && lane == 0) flag_set(ws, FL_F3 + h * 128 + qt * 8, tag);
    STAMP(8);
    chunk_st(cv2, 2);
    if (hw == 0) flag_wait(ws, FL_FP + (4 + 4 * ((lane >> 4) & 1) + (h >> 3)) * 128 + (lane & 15) * 8, lane < 32, tag);
    BARRIER();
    const unsigned lds0 = (unsigned)(uintptr_t)lds;
    const int col0 = 72 * (h & 7), tok0 = 256 * qt + 64 * (h >> 3);
    u32x4 at[3];
#pragma unroll 1
    for (int c = 1; c < 8; ++c) {
        lane = lane_id();
        if (c + 2 < 8) chunk_ld(cv, c + 2);
        if (c == 6) {
#pragma unroll
            for (int e = 0; e < 3; ++e) { const int i = hw + 4 * e; at[e] = ld16(rs, (col0 + 8 * (i < 9 ? i : 0) + (lane >> 3)) * 4096 + tok0 * 2 + (lane & 7) * 16, WS_AT); }
        }
        if (c < 7) {
            const int i0 = (c - 1) * 14, i1 = c < 6 ? i0 + 14 : 81;
#pragma unroll 1
            for (int i = i0 + hw; i < i1; i += 4) {
                const int e = 64 * i + lane, rho = e / 9, ch = e - 9 * rho;
                const int l5 = rho & 31, m = (l5 & 15) >> 1, k5 = (l5 & 16) + (m & 3) + 8 * (m >> 2) + 4 * (l5 & 1);
                glds16(a.wo, wo_row((rho & ~31) + k5) * 1152 + col0 * 2 + ch * 16, lds0 + L_WO + i * 1024);
            }
        }
        if (c + 2 < 8) chunk_st(cv, c + 2);
        if (c == 6 && blockIdx.x == 0 && hw == 3) {
#pragma unroll 1
            for (int i = 0; i < 4; ++i) flag_wait(ws, FL_ST + (64 * i + lane) * 8, true, tag);
            if (lane == 0) flag_set(ws, FL_EPOCH, tag);
        }
        if (c == 5 && hw == 0) flag_wait(ws, FL_F3 + (4 * (h & 7) + ((lane >> 3) & 3)) * 128 + (lane & 7) * 8, lane < 32, tag);
        if (c == 6) {
#pragma unroll
            for (int e = 0; e < 3; ++e) { const int i = hw + 4 * e; if (i < 9) lds_wr128(lds + L_AT + i * 1024 + lane * 16, at[e]); }
        }
        STAMP(8 + c);
        BARRIER();
    }
    if (hw == 0) flag_wait(ws, lane < 32 ? FL_F2 + qt * 256 + lane * 8 : FL_FP + (lane & 7) * 128 + (tok0 >> 7) * 8, lane < 40, tag);
    DRAIN_VM();
    BARRIER();
}

#define DBG_OUT_MODE 0
__device__ __forceinline__ void out_wave(const Args& a, rsrc_t rs, lds_ptr lds, int h, int qt, int w, int lane, char* dbg_ws) {
    const int tt = w & 3, kh = w >> 2, g = lane >> 4, r = lane & 15, q = r >> 2, p = lane & 3;
    const int col0 = 72 * (h & 7), tok0 = 256 * qt + 64 * (h >> 3);
    BARRIER();
    STAMP(18);
    const int t = tok0 + 16 * tt + r;
    u32x4 of[9];
#pragma unroll
    for (int i = 0; i < 9; ++i) {
        const int ks = kh * 9 + i;
        if (ks < 16) of[i] = ld16(rs, (2 * ks + (g >> 1)) * 65536 + t * 32 + (g & 1) * 16, WS_O1);
        else {
            const unsigned b = (16 * (ks - 16) + 4 * g) * 8192 + t * 4;
            of[i] = u32x4{ld4(rs, b, WS_O2), ld4(rs, b + 8192, WS_O2), ld4(rs, b + 16384, WS_O2), ld4(rs, b + 24576, WS_O2)};
        }
    }
    const float gt = __uint_as_float(ld4(rs, t * 4, WS_G));
    f32x4 acc[5];
#pragma unroll
    for (int c = 0; c < 5; ++c) acc[c] = f32x4{0.f, 0.f, 0.f, 0.f};
    lds_cptr wb = (lds_cptr)lds + L_WO + kh * WO_HALF + (16 * (g >> 1) + 2 * (q + 4 * (g & 1))) * 144 + 8 * p;
#pragma unroll
    for (int i = 0; i < 9; ++i) {
#pragma unroll
        for (int c = 0; c < 5; ++c) {
            const u32x2 x = lds_tr(wb + i * 32 * 144 + c * 32), y = lds_tr(wb + i * 32 * 144 + 144 + c * 32);
            acc[c] = mfma16(u32x4{x[0], x[1], y[0], y[1]}, of[i], acc[c]);
        }
    }
    STAMP(19);
    lds_ptr xr = lds + L_RED + tt * 5120 + lane * 16;
#pragma unroll
    for (int c = 0; c < 5; ++c) if ((c < 3) == (kh == 1)) *(LDS_AS f32x4*)(xr + c * 1024) = acc[c];
    BARRIER();
    const float og = bfround(1.0f - gt);
    lds_cptr ab = (lds_cptr)lds + L_AT + q * 128 + (16 * tt + 4 * p) * 2;
#pragma unroll
    for (int c = 0; c < 5; ++c) {
        if ((c < 3) == (kh == 0)) {
            const f32x4 o = *(const LDS_AS f32x4*)(xr + c * 1024);
            const u32x2 av = lds_tr(ab + (16 * c + 4 * g) * 128);
            const float x[4] = {bflo(av[0]), bfhi(av[0]), bflo(av[1]), bfhi(av[1])};
            float v[4];
#pragma unroll
            for (int j = 0; j < 4; ++j) v[j] = bfround(gt * x[j]) + bfround(og * bfround(acc[c][j] + o[j]));
            if (c < 4 || g < 2) *(u32x2*)(a.out + (size_t)t * 1152 + (col0 + 16 * c + 4 * g) * 2) = u32x2{cvtpk(v[0], v[1]), cvtpk(v[2], v[3])};
        }
    }
}

__global__ __launch_bounds__(768, 3) void fused_kernel(Args a) {
    extern __shared__ __attribute__((aligned(16))) char smem[];
    lds_ptr lds = (lds_ptr)smem;
    int lane = threadIdx.x & 63;
    const int w = uni(threadIdx.x >> 6);
    const int xcd = blockIdx.x & 7, slot = blockIdx.x >> 3;
    const int h = xcd * 4 + (slot >> 3), qt = slot & 7;
    char* dbg_ws = a.ws; (void)dbg_ws;
    STAMP(0);
    const rsrc_t rs = __builtin_amdgcn_make_buffer_rsrc(a.ws, 0, WS_END, 0x00020000);
    const u64 ep = __hip_atomic_load((gu64*)(a.ws + FL_EPOCH), RLX_AGENT);
    const int sl = slot - 8, mi = (xcd >> 1) * 4 + (sl & 3), nj = (xcd & 1) * 6 + (sl >> 2), pid = xcd * 8 + slot;

    if (w < 8) {
        const int nch = slot < 8 ? 3 : 2;
        const bool kg = w < 2 * nch;
        const int kd = conv_channel(h, qt, pid, kg ? (w >> 1) : 0), kn = kd * NMODE + (w & 1) * 64 + lane;
        unsigned plar, paim, pcre, pcim, pldt, pd1, pd2, pf0, pf1;
        {
            const char *w0, *w1;
            l2_warm_addr(a, xcd, slot, w, lane, w0, w1);
            asm volatile("global_load_ushort %0, %7, off\n\tglobal_load_ushort %1, %8, off\n\tglobal_load_ushort %2, %9, off\n\tglobal_load_ushort %3, %10, off\n\t"
                         "global_load_ushort %4, %11, off\n\tglobal_load_ushort %5, %12, off\n\tglobal_load_ushort %6, %13, off\n\ts_waitcnt vmcnt(0)"
                         : "=&v"(plar), "=&v"(paim), "=&v"(pcre), "=&v"(pcim), "=&v"(pldt), "=&v"(pd1), "=&v"(pd2)
                         : "v"(a.lar + kn), "v"(a.aim + kn), "v"(a.cre + kn), "v"(a.cim + kn), "v"(a.ldt + kd),
                           "v"(a.dsk + conv_channel(h, qt, pid, w >> 2)), "v"(a.dsk + conv_channel(h, qt, pid, nch - 1))
                         : "memory");
            asm volatile("global_load_dword %0, %2, off\n\tglobal_load_dword %1, %3, off" : "=&{v166}"(pf0), "=&{v167}"(pf1) : "v"(w0), "v"(w1) : "memory");
        }
        const KgPar kp{(unsigned short)plar, (unsigned short)paim, (unsigned short)pcre, (unsigned short)pcim, (unsigned short)pldt};
        const float dsk = bf2f((unsigned short)pd1), dsk2 = bf2f((unsigned short)pd2);
        if (w == 0 && lane < 8) *(LDS_AS unsigned*)(lds + L_CNT + lane * 4) = 0u;
        if (slot < 8) transpose_wave(a, rs, lds, pid, w, lane);
        else if (nj >= 8) { if (w < 4) gemm_wave<true, 0>(a, rs, lds, mi, nj, w, lane, dbg_ws); else gemm_wave<true, 1>(a, rs, lds, mi, nj, w, lane, dbg_ws); }
        else              { if (w < 4) gemm_wave<false, 0>(a, rs, lds, mi, nj, w, lane, dbg_ws); else gemm_wave<false, 1>(a, rs, lds, mi, nj, w, lane, dbg_ws); }
        asm volatile("s_waitcnt vmcnt(0)" : "+{v166}"(pf0), "+{v167}"(pf1) :: "memory");
        const u64 tag = (((u64)(unsigned)uni((int)(ep >> 32)) << 32) | (unsigned)uni((int)ep)) + 1;
        PROBE(1);
        if (slot < 8) { publish(a.ws, lds, 0, 8, FL_FK + pid * 8, tag, lane); BARRIER(); }
        f32x16 kacc = {};
        lds_ptr tab = lds + l_kgt(kg ? w : 0);
        STAMP(21);
        if (kg) {
            kacc = kgen_half(kp, tab, lane);
            if (w & 1) {
#pragma unroll
                for (int i = 0; i < 4; ++i) *(LDS_AS f32x4*)(lds + L_KGP + (w >> 1) * 4096 + i * 1024 + lane * 16) = f32x4{kacc[4 * i], kacc[4 * i + 1], kacc[4 * i + 2], kacc[4 * i + 3]};
            }
        }
        u32x4 uv[6];
        if (w >= 6) {
            flag_wait(a.ws, FL_FK + ((lane & 7) * 8 + xcd) * 8, lane < 8, tag);
#pragma unroll
            for (int i = 0; i < 6; ++i) uv[i] = ld16(rs, (2 * (w - 6) + (i & 1)) * 1024 + lane * 16, WS_UT + conv_channel(h, qt, pid, (i >> 1) < nch ? (i >> 1) : 0) * 4096);
        }
        STAMP(22);
        STAMP(3);
        BARRIER();
        if (w >= 6) {
            if (lane < 60) {
                const int i = (w - 6) * 60 + lane, j = i / 40, k = i - 40 * j;
                lds_wr128(lds + l_cu(j) + (k >> 3) * 1152 + (k & 7) * 16, u32x4{0u, 0u, 0u, 0u});
            }
#pragma unroll
            for (int i = 0; i < 6; ++i) lds_wr128(lds + l_cu(i >> 1) + 128 + (2 * (w - 6) + (i & 1)) * 1152 + lane * 16, uv[i]);
        }
        STAMP(23);
        if (w == 0 && lane == 0) flag_set(a.ws, FL_ST + blockIdx.x * 8, tag);
        if (kg && !(w & 1)) {
#pragma unroll
            for (int i = 0; i < 4; ++i) {
                const f32x4 v = *(const LDS_AS f32x4*)(lds + L_KGP + (w >> 1) * 4096 + i * 1024 + lane * 16);
#pragma unroll
                for (int j = 0; j < 4; ++j) kacc[4 * i + j] += v[j];
            }
            kgen_expand(kacc, lds + l_ck(w >> 1), tab, lane);
        }
        if (w >= 6 || (kg && !(w & 1))) lds_arrive(lds, 3, lane);
        STAMP(24);
        lds_await(lds, 3, nch + 2);
        STAMP(4);
        const int j = w >> 2, tc = 2 * (j ? 3 - (w & 3) : (w & 3));
        lds_ptr scr = lds + L_SCR + w * 4608;
        f32x16 accT, accU;
        lane = lane_id();
        PROBE(2);
        conv_pair(lds, j, tc, lane, accT, accU);
        STAMP(25);
        const f32x4 ya = conv_close(accT, lds, scr, j, tc, lane), yb = conv_close(accU, lds, scr, j, tc + 1, lane);
        f32x4 yc = {}, yd = {};
        const bool third = nch == 3 && w < 4;
        if (third) {
            lane = lane_id();
            conv_pair(lds, 2, 2 * w, lane, accT, accU);
            yc = conv_close(accT, lds, scr, 2, 2 * w, lane); yd = conv_close(accU, lds, scr, 2, 2 * w + 1, lane);
        }
        lds_arrive(lds, 6, lane);
        STAMP(5);
        const int q0 = qt * 256 + (w & 3) * 64;
        lds_await(lds, 4, 1);
        PROBE(3);
        const u32x4 x = ld16(rs, (q0 >> 5) * 1024 + lane * 16, WS_Q1 + h * 65536), y = ld16(rs, (q0 >> 5) * 1024 + 1024 + lane * 16, WS_Q1 + h * 65536);
        const unsigned q2a = ld4(rs, (q0 + lane) * 8, WS_Q2 + h * 16384);
        lds_await(lds, 6, 8);
        {
            const int d = conv_channel(h, qt, pid, j);
            conv_finish(rs, lds, j, d, dsk, tc, ya, lane); conv_finish(rs, lds, j, d, dsk, tc + 1, yb, lane);
            if (third) { const int d2 = conv_channel(h, qt, pid, 2); conv_finish(rs, lds, 2, d2, dsk2, 2 * w, yc, lane); conv_finish(rs, lds, 2, d2, dsk2, 2 * w + 1, yd, lane); }
        }
        STAMP(26);
        BARRIER();
        STAMP(27);
        PROBE(4);
        lane = lane_id();
        attn_wave(a, rs, lds, h, qt, w, lane, u32x8{x[0], x[1], x[2], x[3], y[0], y[1], y[2], y[3]}, u32x2{q2a, 0u}, tag, dbg_ws);
        STAMP(16);
        PROBE(5);
        lane = lane_id();
        out_wave(a, rs, lds, h, qt, w, lane, dbg_ws);
        STAMP(20);
    } else {
        const int hw = w - 8;
        if (slot < 8) transpose_wave(a, rs, lds, pid, w, lane);
        else gemm_loader(a, lds, mi, nj, hw, lane, dbg_ws);
        const u64 tag = (((u64)(unsigned)uni((int)(ep >> 32)) << 32) | (unsigned)uni((int)ep)) + 1;
        if (slot >= 8) gemm_store(rs, lds, mi, nj, hw, lane); else { BARRIER(); BARRIER(); }
        helper_wave(a, rs, lds, h, qt, hw, lane, slot < 8 ? 0u : FL_FP + nj * 128 + mi * 8, tag, dbg_ws);
        STAMP(20);
    }
}

extern "C" void kernel_launch(void* const* d_in, const int* in_sizes, int n_in, void* d_out, int out_size, void* d_ws, size_t ws_size,
                              hipStream_t stream) {
    (void)in_sizes; (void)n_in; (void)out_size; (void)ws_size;
    Args a;
    a.x = (const char*)d_in[0]; a.wqkv = (const char*)d_in[8]; a.wo = (const char*)d_in[9]; a.gate = (const char*)d_in[10];
    a.lar = (const unsigned short*)d_in[1]; a.aim = (const unsigned short*)d_in[2]; a.cre = (const unsigned short*)d_in[3];
    a.cim = (const unsigned short*)d_in[4]; a.ldt = (const unsigned short*)d_in[5]; a.dsk = (const unsigned short*)d_in[6];
    a.ws = (char*)d_ws; a.out = (char*)d_out;
    (void)hipFuncSetAttribute((const void*)fused_kernel, hipFuncAttributeMaxDynamicSharedMemorySize, LDS_BYTES);
    fused_kernel<<<256, 768, LDS_BYTES, stream>>>(a);
}
```
